```python
import torch

def _softmax(x, axis=-1):
    return torch.softmax(x, dim=axis)
def _gelu(x):
    return torch.nn.functional.gelu(x, approximate="tanh")
def _silu(x):
    return torch.nn.functional.silu(x)
def _sigmoid(x):
    return torch.sigmoid(x)
def _layernorm(x, axis=-1, eps=1e-5):
    m = x.mean(dim=axis, keepdim=True)
    v = x.var(dim=axis, keepdim=True, unbiased=False)
    return (x - m) / torch.sqrt(v + eps)
def _scatter_add(tgt, idx, src, axis=0):
    return tgt.clone().index_add_(axis, idx, src)
def _segment_sum(src, seg, n):
    out = torch.zeros((n,) + src.shape[1:], dtype=src.dtype)
    return out.index_add_(0, seg, src)
def _pad(x, pw):
    flat = tuple(v for pair in reversed(pw) for v in pair)
    return torch.nn.functional.pad(x, flat)

kernel_name = 'standalone__gqa_softmax__near_ridge'

def setup_inputs():

    _w = (lambda a: (lambda t: t.to(torch.bfloat16) if t.is_floating_point() else t)(torch.as_tensor(a)))
    B, Hq, Hkv, Sq, Sk, D = 8, 32, 32, 240, 1024, 128
    return {
        "q": _w((lambda *s: torch.randn(*s, dtype=torch.bfloat16) * 0.8)(B, Hq, Sq, D)),
        "k": _w((lambda *s: torch.randn(*s, dtype=torch.bfloat16) * 0.8)(B, Hkv, Sk, D)),
        "v": _w((lambda *s: torch.randn(*s, dtype=torch.bfloat16) * 0.05)(B, Hkv, Sk, D)),
    }

def reference(q, k, v):
    B, Hq, Sq, D = q.shape
    Hkv = k.shape[1]
    rep = Hq // Hkv
    k = (lambda x, n, axis: torch.repeat_interleave(x, n, dim=axis))(k, rep, axis=1)
    v = (lambda x, n, axis: torch.repeat_interleave(x, n, dim=axis))(v, rep, axis=1)
    scale = D ** -0.5
    scores = torch.einsum("bhqd,bhkd->bhqk", q, k) * scale
    p = _softmax(scores, axis=-1)
    return torch.einsum("bhqk,bhkd->bhqd", p, v)
```

```cpp
#include <hip/hip_runtime.h>
#include <hip/hip_bf16.h>
#include <cstdio>

using bf16 = __hip_bfloat16;
constexpr int   D = 128, NW = 8, QBLK = 32, KVBLK = 64;
constexpr float SCALE = 0.088388347648318440f;
constexpr float THR = 8.f;
constexpr int SDEPTH = 2;
constexpr int B = 8, HQ = 32, HKV = 32, SQ = 240, SKV = 1024;
constexpr int LDQ = D, LDK = D, LDO = D;
constexpr size_t SHM_V = KVBLK * D * 2, SHM_K = KVBLK * D * 2, SHM_ATTN = 2 * SHM_V + 2 * SHM_K + NW * 64 * 4;

using bf16x8 = __attribute__((ext_vector_type(8))) short;
using s16x4  = __attribute__((ext_vector_type(4))) short;
using f32x16 = __attribute__((ext_vector_type(16))) float;
using f32x8  = __attribute__((ext_vector_type(8))) float;
using u32x4  = __attribute__((ext_vector_type(4))) unsigned;
#define KSWZ(row, colB) ((row) * 256 + ((colB) ^ (((row) & 7) << 4)))
#define SBAR() __builtin_amdgcn_sched_barrier(0)
__device__ __forceinline__ int crow(int r, int hi) { return (r & 3) + 8 * (r >> 2) + 4 * hi; }
__device__ __forceinline__ unsigned cvtpk(float lo, float hi) {
  unsigned r; asm volatile("v_cvt_pk_bf16_f32 %0, %1, %2" : "=v"(r) : "v"(lo), "v"(hi)); return r;
}
__device__ __forceinline__ bf16x8 ld8(const bf16* p) { return *reinterpret_cast<const bf16x8*>(p); }

__device__ __forceinline__ void partialSM(f32x16& p0, f32x16& p1, float& m_reg, float& mn, float& alpha) {
  constexpr float C = SCALE * 1.4426950408889634f;
  float pmax = p0[0]; for (int r = 1; r < 16; ++r) pmax = fmaxf(pmax, p0[r]); for (int r = 0; r < 16; ++r) pmax = fmaxf(pmax, p1[r]);
  { auto rr = __builtin_amdgcn_permlane32_swap(__float_as_uint(pmax), __float_as_uint(pmax), false, false);
    pmax = fmaxf(__uint_as_float(rr[0]), __uint_as_float(rr[1])); }
  if (__builtin_expect(__all(pmax - m_reg <= THR / SCALE), 1)) { mn = m_reg; alpha = 1.f; }
  else { mn = fmaxf(m_reg, pmax); alpha = __builtin_amdgcn_exp2f((m_reg - mn) * C); m_reg = mn; }
  float mnC = -mn * C;
  for (int r = 0; r < 16; ++r) p0[r] = fmaf(p0[r], C, mnC); for (int r = 0; r < 16; ++r) p1[r] = fmaf(p1[r], C, mnC);
  for (int r = 0; r < 16; ++r) p0[r] = __builtin_amdgcn_exp2f(p0[r]);
}
__device__ __forceinline__ void finishSM(f32x16& p0, f32x16& p1, float alpha, float& l_reg, bf16x8& pa0, bf16x8& pa1, bf16x8& pa2, bf16x8& pa3) {
  for (int r = 0; r < 16; ++r) p1[r] = __builtin_amdgcn_exp2f(p1[r]);
  float ps = 0; for (int r = 0; r < 16; ++r) ps += p0[r]; for (int r = 0; r < 16; ++r) ps += p1[r];
  { auto rr = __builtin_amdgcn_permlane32_swap(__float_as_uint(ps), __float_as_uint(ps), false, false);
    ps = __uint_as_float(rr[0]) + __uint_as_float(rr[1]); }
  l_reg = l_reg * alpha + ps;
#define PK4(P, BASE, OUT) do { unsigned a0 = cvtpk(P[BASE + 0], P[BASE + 1]), a1 = cvtpk(P[BASE + 2], P[BASE + 3]);   \
    unsigned b0 = cvtpk(P[BASE + 4], P[BASE + 5]), b1 = cvtpk(P[BASE + 6], P[BASE + 7]);                              \
    auto r0 = __builtin_amdgcn_permlane32_swap(a0, b0, false, false); auto r1 = __builtin_amdgcn_permlane32_swap(a1, b1, false, false); \
    u32x4 w = {r0[0], r1[0], r0[1], r1[1]}; OUT = *reinterpret_cast<bf16x8*>(&w); } while (0)
  PK4(p0, 0, pa0); PK4(p0, 8, pa1); PK4(p1, 0, pa2); PK4(p1, 8, pa3);
#undef PK4
}
__device__ __forceinline__ void qkt(f32x16& p0, f32x16& p1, const bf16* Ks, const bf16x8* qr, int r32, int hi) {
  p0 = f32x16{}; p1 = f32x16{};
  for (int d0 = 0; d0 < 8; ++d0) { int cb = (d0 * 16 + hi * 8) * 2;
    bf16x8 b0 = *reinterpret_cast<const bf16x8*>((const char*)Ks + KSWZ(r32, cb));
    bf16x8 b1 = *reinterpret_cast<const bf16x8*>((const char*)Ks + KSWZ(32 + r32, cb));
    p0 = __builtin_amdgcn_mfma_f32_32x32x16_bf16(b0, qr[d0], p0, 0, 0, 0);
    p1 = __builtin_amdgcn_mfma_f32_32x32x16_bf16(b1, qr[d0], p1, 0, 0, 0); }
}
__device__ __forceinline__ int v_st(int k, int c) { const int kk = (k & ~0xC) | ((k & 4) << 1) | ((k & 8) >> 1); return ((kk >> 3) * 4 + (c >> 5)) * 512 + ((kk & 7) * 32 + (c & 31)) * 2; }
__device__ __forceinline__ int v_rd_base(int lane) { return ((lane & 3) << 3) | (((lane >> 2) & 3) << 6) | (((lane >> 4) & 1) << 5) | (((lane >> 5) & 1) << 8); }
constexpr int v_rd_off(int d0, int ks, int half) { return d0 * 512 + ks * 4096 + half * 2048; }
template <int OFF> __device__ __forceinline__ s16x4 tr_read(int vb) {
  s16x4 r; asm volatile("ds_read_b64_tr_b16 %0, %1 offset:%2" : "=&v"(r) : "v"(vb), "i"(OFF) : "memory"); return r;
}
template <int D0> __device__ __forceinline__ void pv_one(f32x16& od, int vb, bf16x8 pa0, bf16x8 pa1, bf16x8 pa2, bf16x8 pa3) {
  const s16x4 l0 = tr_read<v_rd_off(D0, 0, 0)>(vb), h0 = tr_read<v_rd_off(D0, 0, 1)>(vb), l1 = tr_read<v_rd_off(D0, 1, 0)>(vb), h1 = tr_read<v_rd_off(D0, 1, 1)>(vb);
  const s16x4 l2 = tr_read<v_rd_off(D0, 2, 0)>(vb), h2 = tr_read<v_rd_off(D0, 2, 1)>(vb), l3 = tr_read<v_rd_off(D0, 3, 0)>(vb), h3 = tr_read<v_rd_off(D0, 3, 1)>(vb);
  asm volatile("s_waitcnt lgkmcnt(0)" ::: "memory"); SBAR();
#define PK(L, H) (bf16x8){L[0], L[1], L[2], L[3], H[0], H[1], H[2], H[3]}
  od = __builtin_amdgcn_mfma_f32_32x32x16_bf16(pa0, PK(l0, h0), od, 0, 0, 0);
  od = __builtin_amdgcn_mfma_f32_32x32x16_bf16(pa1, PK(l1, h1), od, 0, 0, 0);
  od = __builtin_amdgcn_mfma_f32_32x32x16_bf16(pa2, PK(l2, h2), od, 0, 0, 0);
  od = __builtin_amdgcn_mfma_f32_32x32x16_bf16(pa3, PK(l3, h3), od, 0, 0, 0);
#undef PK
}
__device__ __forceinline__ void pv_d0(f32x16* o, int vb, bf16x8 pa0, bf16x8 pa1, bf16x8 pa2, bf16x8 pa3) {
  pv_one<0>(o[0], vb, pa0, pa1, pa2, pa3); pv_one<1>(o[1], vb, pa0, pa1, pa2, pa3); pv_one<2>(o[2], vb, pa0, pa1, pa2, pa3); pv_one<3>(o[3], vb, pa0, pa1, pa2, pa3);
}

__device__ __forceinline__ void attn_dense_body(const bf16* __restrict__ Qb, const bf16* __restrict__ Kh, const bf16* __restrict__ Vh,
                                                bf16* __restrict__ Ob, int seq, char* lds) {
  const int tid = threadIdx.x, wid = tid >> 6, lane = tid & 63, r32 = lane & 31, hi = lane >> 5;
  bf16* V_lds = (bf16*)lds; bf16* K_lds = (bf16*)(lds + 2 * SHM_V);
  float* ws = (float*)(lds + 2 * SHM_V + 2 * SHM_K) + wid * 64; float* li_l = ws; float* al_l = ws + 32;
  float m_reg = -1e30f, l_reg = 0; f32x16 o[4] = {}; bf16x8 qr[8];
  int qrow = wid * QBLK + r32; if (qrow > SQ - 1) qrow = SQ - 1;
  const bf16* Qw = Qb + (long)qrow * LDQ + hi * 8;
#pragma unroll
  for (int d0 = 0; d0 < 8; ++d0) qr[d0] = ld8(Qw + d0 * 16);
  const int sr = tid >> 4, sc = (tid & 15) * 8, vst0 = v_st(sr, sc), vst1 = v_st(32 + sr, sc);
  const int vb0 = (int)(uintptr_t)V_lds + v_rd_base(lane);
  struct { bf16x8 vs0, vs1, ks0, ks1; } sr_[SDEPTH];
#define SLOAD(i, k0) do { sr_[i].vs0 = ld8(&Vh[(long)((k0) + sr) * LDK + sc]); sr_[i].vs1 = ld8(&Vh[(long)((k0) + 32 + sr) * LDK + sc]); \
    sr_[i].ks0 = ld8(&Kh[(long)((k0) + sr) * LDK + sc]); sr_[i].ks1 = ld8(&Kh[(long)((k0) + 32 + sr) * LDK + sc]); } while (0)
#define SWRITE(b, i) do { *(bf16x8*)((char*)V_lds + (b) * SHM_V + vst0) = sr_[i].vs0;          \
    *(bf16x8*)((char*)V_lds + (b) * SHM_V + vst1) = sr_[i].vs1; int kc = sc * 2;               \
    *(bf16x8*)((char*)K_lds + (b) * SHM_K + KSWZ(sr, kc)) = sr_[i].ks0;                       \
    *(bf16x8*)((char*)K_lds + (b) * SHM_K + KSWZ(32 + sr, kc)) = sr_[i].ks1; } while (0)
#define SWAIT() do { asm volatile("s_waitcnt vmcnt(4)" ::: "memory"); } while (0)
#define RESC(a) do { if (__any((a) < 1.f)) { if (hi == 0) al_l[r32] = (a); asm volatile("s_waitcnt lgkmcnt(0)" ::: "memory"); \
    for (int d = 0; d < 4; ++d) for (int r = 0; r < 16; ++r) o[d][r] *= al_l[crow(r, hi)]; } } while (0)
  f32x16 pA0, pA1, pB0, pB1; float mnA, mnB, alA, alB; bf16x8 pa0, pa1, pa2, pa3; const int NT = seq / KVBLK;
  constexpr int SE = 0, SO = SDEPTH - 1;
  SLOAD(SE, 0); asm volatile("s_waitcnt vmcnt(0)" ::: "memory"); SWRITE(0, SE); __syncthreads();
  qkt(pA0, pA1, K_lds, qr, r32, hi); partialSM(pA0, pA1, m_reg, mnA, alA);
  SLOAD(SO, KVBLK); SLOAD(SE, 2 * KVBLK);
  SWAIT(); SWRITE(1, SO); __syncthreads();
  for (int j = 1; j + 1 < NT; j += 2) {
    SBAR(); qkt(pB0, pB1, (bf16*)((char*)K_lds + SHM_K), qr, r32, hi);
    finishSM(pA0, pA1, alA, l_reg, pa0, pa1, pa2, pa3); SBAR();
    SLOAD(SO, (j + SDEPTH) * KVBLK); SBAR();
    pv_d0(o, vb0, pa0, pa1, pa2, pa3); partialSM(pB0, pB1, m_reg, mnB, alB);
    __syncthreads(); SWAIT(); SWRITE(0, SE);
    RESC(alB); __syncthreads();
    SBAR(); qkt(pA0, pA1, K_lds, qr, r32, hi);
    finishSM(pB0, pB1, alB, l_reg, pa0, pa1, pa2, pa3); SBAR();
    if (j + 3 < NT) SLOAD(SE, (j + 1 + SDEPTH) * KVBLK); SBAR();
    pv_d0(o, vb0 + (int)SHM_V, pa0, pa1, pa2, pa3); partialSM(pA0, pA1, m_reg, mnA, alA);
    __syncthreads(); SWAIT(); SWRITE(1, SO);
    RESC(alA); __syncthreads();
  }
  SBAR(); qkt(pB0, pB1, (bf16*)((char*)K_lds + SHM_K), qr, r32, hi);
  finishSM(pA0, pA1, alA, l_reg, pa0, pa1, pa2, pa3); SBAR();
  pv_d0(o, vb0, pa0, pa1, pa2, pa3); partialSM(pB0, pB1, m_reg, mnB, alB);
  __syncthreads(); RESC(alB);
  finishSM(pB0, pB1, alB, l_reg, pa0, pa1, pa2, pa3); SBAR();
  pv_d0(o, vb0 + (int)SHM_V, pa0, pa1, pa2, pa3);
  if (hi == 0) li_l[r32] = l_reg; asm volatile("s_waitcnt lgkmcnt(0)" ::: "memory");
  float rli[16];
#pragma unroll
  for (int r = 0; r < 16; ++r) rli[r] = __builtin_amdgcn_rcpf(li_l[crow(r, hi)]);
  __syncthreads();
  unsigned short* ost = (unsigned short*)(lds + wid * 8192);
#pragma unroll
  for (int r = 0; r < 16; ++r) { int orow = crow(r, hi);
    for (int d0 = 0; d0 < 4; ++d0) { unsigned pk = cvtpk(o[d0][r] * rli[r], 0.f); ost[orow * 128 + d0 * 32 + r32] = (unsigned short)pk; } }
  asm volatile("s_waitcnt lgkmcnt(0)" ::: "memory");
  bf16* Ow = Ob + (long)(wid * QBLK) * LDO;
#pragma unroll
  for (int i = 0; i < 8; ++i) { int e = i * 64 + lane; int row = e >> 4, c8 = (e & 15) * 8;
    bf16x8 v = *reinterpret_cast<const bf16x8*>(ost + row * 128 + c8);
    if (wid * QBLK + row < SQ) *reinterpret_cast<bf16x8*>(Ow + (long)row * LDO + c8) = v; }
#undef SLOAD
#undef SWRITE
#undef SWAIT
#undef RESC
}

__global__ __launch_bounds__(64 * NW, 1)
void attn_fwd_dense(const bf16* __restrict__ Q, const bf16* __restrict__ K, const bf16* __restrict__ V, bf16* __restrict__ O) {
  extern __shared__ __attribute__((aligned(16))) char shm[];
  const long bh = blockIdx.x;
  attn_dense_body(Q + bh * SQ * D, K + bh * SKV * (long)D, V + bh * SKV * (long)D, O + bh * SQ * D, SKV, shm);
}

extern "C" void kernel_launch(void* const* d_in, const int* in_sizes, int n_in, void* d_out, int out_size, void* d_ws, size_t ws_size, hipStream_t stream) {
  hipFuncSetAttribute((const void*)attn_fwd_dense, hipFuncAttributeMaxDynamicSharedMemorySize, (int)SHM_ATTN);
  hipLaunchKernelGGL(attn_fwd_dense, dim3(B * HQ), dim3(64 * NW), SHM_ATTN, stream,
                     (const bf16*)d_in[0], (const bf16*)d_in[1], (const bf16*)d_in[2], (bf16*)d_out);
}
```

```cpp
#include <hip/hip_runtime.h>
#include <hip/hip_bf16.h>

extern "C" __global__ __launch_bounds__(256, 1)
void gqa_attn_kernel(const unsigned short* __restrict__ q, const unsigned short* __restrict__ k,
                     const unsigned short* __restrict__ v, unsigned short* __restrict__ o) {
  __shared__ __attribute__((aligned(16))) char lds[131072];
  const unsigned bh = blockIdx.x;
  auto srdQ = __builtin_amdgcn_make_buffer_rsrc((void*)(q + (size_t)bh * 240 * 128), (short)0, 240 * 256, 0x00020000);
  auto srdK = __builtin_amdgcn_make_buffer_rsrc((void*)(k + (size_t)bh * 1024 * 128), (short)0, 1024 * 256, 0x00020000);
  auto srdV = __builtin_amdgcn_make_buffer_rsrc((void*)(v + (size_t)bh * 1024 * 128), (short)0, 1024 * 256, 0x00020000);
  auto srdO = __builtin_amdgcn_make_buffer_rsrc((void*)(o + (size_t)bh * 240 * 128), (short)0, 240 * 256, 0x00020000);
  unsigned tid = threadIdx.x;
  asm volatile("" :: "v"(lds));
  asm volatile(
    "v_and_b32 v253, 63, %4\n\t"
    "v_lshrrev_b32 v248, 6, %4\n\t"
    "s_nop 0\n\t"
    "v_readfirstlane_b32 s47, v248\n\t"
    "s_lshl_b32 s40, s47, 12\n\t"
    "s_mov_b32 s44, 0x41000000\n\t"
    "s_movk_i32 s45, 0x2000\n\t"
    "s_mov_b32 s46, 0\n\t"
    "v_and_b32 v254, 31, v253\n\t"
    "v_lshrrev_b32 v247, 5, v253\n\t"
    "v_lshlrev_b32 v248, 8, v254\n\t"
    "v_lshl_add_u32 v248, v247, 4, v248\n\t"
    "s_lshl_b32 s47, s40, 2\n\t"
    "v_add_u32 v241, s47, v248\n\t"
    "v_and_b32 v249, 15, v254\n\t"
    "v_lshlrev_b32 v250, 8, v254\n\t"
    "v_or_b32 v251, 0, v247\n\t"
    "v_xor_b32 v251, v251, v249\n\t"
    "v_lshl_add_u32 v224, v251, 4, v250\n\t"
    "v_or_b32 v251, 2, v247\n\t"
    "v_xor_b32 v251, v251, v249\n\t"
    "v_lshl_add_u32 v225, v251, 4, v250\n\t"
    "v_or_b32 v251, 4, v247\n\t"
    "v_xor_b32 v251, v251, v249\n\t"
    "v_lshl_add_u32 v226, v251, 4, v250\n\t"
    "v_or_b32 v251, 6, v247\n\t"
    "v_xor_b32 v251, v251, v249\n\t"
    "v_lshl_add_u32 v227, v251, 4, v250\n\t"
    "v_or_b32 v251, 8, v247\n\t"
    "v_xor_b32 v251, v251, v249\n\t"
    "v_lshl_add_u32 v228, v251, 4, v250\n\t"
    "v_or_b32 v251, 10, v247\n\t"
    "v_xor_b32 v251, v251, v249\n\t"
    "v_lshl_add_u32 v229, v251, 4, v250\n\t"
    "v_or_b32 v251, 12, v247\n\t"
    "v_xor_b32 v251, v251, v249\n\t"
    "v_lshl_add_u32 v230, v251, 4, v250\n\t"
    "v_or_b32 v251, 14, v247\n\t"
    "v_xor_b32 v251, v251, v249\n\t"
    "v_lshl_add_u32 v231, v251, 4, v250\n\t"
    "v_bfe_u32 v249, v253, 2, 2\n\t"
    "v_bfe_u32 v250, v253, 4, 1\n\t"
    "v_and_b32 v251, 3, v253\n\t"
    "v_lshlrev_b32 v252, 10, v247\n\t"
    "v_lshl_add_u32 v252, v249, 8, v252\n\t"
    "v_lshl_add_u32 v252, v250, 5, v252\n\t"
    "v_lshl_add_u32 v252, v251, 3, v252\n\t"
    "v_add_u32 v252, 0x10000, v252\n\t"
    "v_xor_b32 v248, 0, v249\n\t"
    "v_lshl_add_u32 v232, v248, 6, v252\n\t"
    "v_xor_b32 v248, 1, v249\n\t"
    "v_lshl_add_u32 v233, v248, 6, v252\n\t"
    "v_xor_b32 v248, 2, v249\n\t"
    "v_lshl_add_u32 v234, v248, 6, v252\n\t"
    "v_xor_b32 v248, 3, v249\n\t"
    "v_lshl_add_u32 v235, v248, 6, v252\n\t"
    "v_lshrrev_b32 v249, 4, v253\n\t"
    "v_and_b32 v250, 15, v253\n\t"
    "v_or_b32 v251, 0, v249\n\t"
    "v_xor_b32 v251, v251, v250\n\t"
    "v_lshlrev_b32 v251, 4, v251\n\t"
    "v_lshl_add_u32 v236, v249, 8, v251\n\t"
    "v_or_b32 v251, 4, v249\n\t"
    "v_xor_b32 v251, v251, v250\n\t"
    "v_lshlrev_b32 v251, 4, v251\n\t"
    "v_lshl_add_u32 v237, v249, 8, v251\n\t"
    "v_or_b32 v251, 8, v249\n\t"
    "v_xor_b32 v251, v251, v250\n\t"
    "v_lshlrev_b32 v251, 4, v251\n\t"
    "v_lshl_add_u32 v238, v249, 8, v251\n\t"
    "v_or_b32 v251, 12, v249\n\t"
    "v_xor_b32 v251, v251, v250\n\t"
    "v_lshlrev_b32 v251, 4, v251\n\t"
    "v_lshl_add_u32 v239, v249, 8, v251\n\t"
    "v_lshlrev_b32 v251, 2, v249\n\t"
    "v_xor_b32 v251, v251, v250\n\t"
    "v_lshlrev_b32 v251, 4, v251\n\t"
    "v_lshl_add_u32 v240, v249, 8, v251\n\t"
    "s_mov_b32 s41, s40\n\t"
    "s_mov_b32 s42, s40\n\t"
    "buffer_load_dwordx4 v[0:3], v241, %0, 0 offen offset:0\n\t"
    "buffer_load_dwordx4 v[4:7], v241, %0, 0 offen offset:32\n\t"
    "buffer_load_dwordx4 v[8:11], v241, %0, 0 offen offset:64\n\t"
    "buffer_load_dwordx4 v[12:15], v241, %0, 0 offen offset:96\n\t"
    "buffer_load_dwordx4 v[16:19], v241, %0, 0 offen offset:128\n\t"
    "buffer_load_dwordx4 v[20:23], v241, %0, 0 offen offset:160\n\t"
    "buffer_load_dwordx4 v[24:27], v241, %0, 0 offen offset:192\n\t"
    "buffer_load_dwordx4 v[28:31], v241, %0, 0 offen offset:224\n\t"
    "buffer_load_dwordx4 v[32:35], v241, %0, s45 offen offset:0\n\t"
    "buffer_load_dwordx4 v[36:39], v241, %0, s45 offen offset:32\n\t"
    "buffer_load_dwordx4 v[40:43], v241, %0, s45 offen offset:64\n\t"
    "buffer_load_dwordx4 v[44:47], v241, %0, s45 offen offset:96\n\t"
    "buffer_load_dwordx4 v[48:51], v241, %0, s45 offen offset:128\n\t"
    "buffer_load_dwordx4 v[52:55], v241, %0, s45 offen offset:160\n\t"
    "buffer_load_dwordx4 v[56:59], v241, %0, s45 offen offset:192\n\t"
    "buffer_load_dwordx4 v[60:63], v241, %0, s45 offen offset:224\n\t"
    "s_add_u32 m0, s40, 0x0\n\t"
    "s_nop 0\n\t"
    "buffer_load_dwordx4 v236, %1, s41 offen offset:0 nt lds\n\t"
    "buffer_load_dwordx4 v237, %1, s41 offen offset:1024 nt lds\n\t"
    "buffer_load_dwordx4 v238, %1, s41 offen offset:2048 nt lds\n\t"
    "buffer_load_dwordx4 v239, %1, s41 offen offset:3072 nt lds\n\t"
    "s_add_u32 s41, s41, 0x4000\n\t"
    "s_add_u32 m0, s40, 0x4000\n\t"
    "s_nop 0\n\t"
    "buffer_load_dwordx4 v236, %1, s41 offen offset:0 nt lds\n\t"
    "buffer_load_dwordx4 v237, %1, s41 offen offset:1024 nt lds\n\t"
    "buffer_load_dwordx4 v238, %1, s41 offen offset:2048 nt lds\n\t"
    "buffer_load_dwordx4 v239, %1, s41 offen offset:3072 nt lds\n\t"
    "s_add_u32 s41, s41, 0x4000\n\t"
    "s_add_u32 m0, s40, 0x10000\n\t"
    "s_nop 0\n\t"
    "buffer_load_dwordx4 v240, %2, s42 offen offset:0 nt lds\n\t"
    "buffer_load_dwordx4 v240, %2, s42 offen offset:1024 nt lds\n\t"
    "buffer_load_dwordx4 v240, %2, s42 offen offset:2048 nt lds\n\t"
    "buffer_load_dwordx4 v240, %2, s42 offen offset:3072 nt lds\n\t"
    "s_add_u32 s42, s42, 0x4000\n\t"
    "s_add_u32 m0, s40, 0x8000\n\t"
    "s_nop 0\n\t"
    "buffer_load_dwordx4 v236, %1, s41 offen offset:0 nt lds\n\t"
    "buffer_load_dwordx4 v237, %1, s41 offen offset:1024 nt lds\n\t"
    "buffer_load_dwordx4 v238, %1, s41 offen offset:2048 nt lds\n\t"
    "buffer_load_dwordx4 v239, %1, s41 offen offset:3072 nt lds\n\t"
    "s_add_u32 s41, s41, 0x4000\n\t"
    "s_add_u32 m0, s40, 0x14000\n\t"
    "s_nop 0\n\t"
    "buffer_load_dwordx4 v240, %2, s42 offen offset:0 nt lds\n\t"
    "buffer_load_dwordx4 v240, %2, s42 offen offset:1024 nt lds\n\t"
    "buffer_load_dwordx4 v240, %2, s42 offen offset:2048 nt lds\n\t"
    "buffer_load_dwordx4 v240, %2, s42 offen offset:3072 nt lds\n\t"
    "s_add_u32 s42, s42, 0x4000\n\t"
    "s_add_u32 m0, s40, 0xc000\n\t"
    "s_nop 0\n\t"
    "buffer_load_dwordx4 v236, %1, s41 offen offset:0 nt lds\n\t"
    "buffer_load_dwordx4 v237, %1, s41 offen offset:1024 nt lds\n\t"
    "buffer_load_dwordx4 v238, %1, s41 offen offset:2048 nt lds\n\t"
    "buffer_load_dwordx4 v239, %1, s41 offen offset:3072 nt lds\n\t"
    "s_add_u32 s41, s41, 0x4000\n\t"
    "s_add_u32 m0, s40, 0x18000\n\t"
    "s_nop 0\n\t"
    "buffer_load_dwordx4 v240, %2, s42 offen offset:0 nt lds\n\t"
    "buffer_load_dwordx4 v240, %2, s42 offen offset:1024 nt lds\n\t"
    "buffer_load_dwordx4 v240, %2, s42 offen offset:2048 nt lds\n\t"
    "buffer_load_dwordx4 v240, %2, s42 offen offset:3072 nt lds\n\t"
    "s_add_u32 s42, s42, 0x4000\n\t"
    "v_mov_b32 v248, 0\n\t"
    "v_accvgpr_write_b32 a0, v248\n\t"
    "v_accvgpr_write_b32 a1, v248\n\t"
    "v_accvgpr_write_b32 a2, v248\n\t"
    "v_accvgpr_write_b32 a3, v248\n\t"
    "v_accvgpr_write_b32 a4, v248\n\t"
    "v_accvgpr_write_b32 a5, v248\n\t"
    "v_accvgpr_write_b32 a6, v248\n\t"
    "v_accvgpr_write_b32 a7, v248\n\t"
    "v_accvgpr_write_b32 a8, v248\n\t"
    "v_accvgpr_write_b32 a9, v248\n\t"
    "v_accvgpr_write_b32 a10, v248\n\t"
    "v_accvgpr_write_b32 a11, v248\n\t"
    "v_accvgpr_write_b32 a12, v248\n\t"
    "v_accvgpr_write_b32 a13, v248\n\t"
    "v_accvgpr_write_b32 a14, v248\n\t"
    "v_accvgpr_write_b32 a15, v248\n\t"
    "v_accvgpr_write_b32 a16, v248\n\t"
    "v_accvgpr_write_b32 a17, v248\n\t"
    "v_accvgpr_write_b32 a18, v248\n\t"
    "v_accvgpr_write_b32 a19, v248\n\t"
    "v_accvgpr_write_b32 a20, v248\n\t"
    "v_accvgpr_write_b32 a21, v248\n\t"
    "v_accvgpr_write_b32 a22, v248\n\t"
    "v_accvgpr_write_b32 a23, v248\n\t"
    "v_accvgpr_write_b32 a24, v248\n\t"
    "v_accvgpr_write_b32 a25, v248\n\t"
    "v_accvgpr_write_b32 a26, v248\n\t"
    "v_accvgpr_write_b32 a27, v248\n\t"
    "v_accvgpr_write_b32 a28, v248\n\t"
    "v_accvgpr_write_b32 a29, v248\n\t"
    "v_accvgpr_write_b32 a30, v248\n\t"
    "v_accvgpr_write_b32 a31, v248\n\t"
    "v_accvgpr_write_b32 a32, v248\n\t"
    "v_accvgpr_write_b32 a33, v248\n\t"
    "v_accvgpr_write_b32 a34, v248\n\t"
    "v_accvgpr_write_b32 a35, v248\n\t"
    "v_accvgpr_write_b32 a36, v248\n\t"
    "v_accvgpr_write_b32 a37, v248\n\t"
    "v_accvgpr_write_b32 a38, v248\n\t"
    "v_accvgpr_write_b32 a39, v248\n\t"
    "v_accvgpr_write_b32 a40, v248\n\t"
    "v_accvgpr_write_b32 a41, v248\n\t"
    "v_accvgpr_write_b32 a42, v248\n\t"
    "v_accvgpr_write_b32 a43, v248\n\t"
    "v_accvgpr_write_b32 a44, v248\n\t"
    "v_accvgpr_write_b32 a45, v248\n\t"
    "v_accvgpr_write_b32 a46, v248\n\t"
    "v_accvgpr_write_b32 a47, v248\n\t"
    "v_accvgpr_write_b32 a48, v248\n\t"
    "v_accvgpr_write_b32 a49, v248\n\t"
    "v_accvgpr_write_b32 a50, v248\n\t"
    "v_accvgpr_write_b32 a51, v248\n\t"
    "v_accvgpr_write_b32 a52, v248\n\t"
    "v_accvgpr_write_b32 a53, v248\n\t"
    "v_accvgpr_write_b32 a54, v248\n\t"
    "v_accvgpr_write_b32 a55, v248\n\t"
    "v_accvgpr_write_b32 a56, v248\n\t"
    "v_accvgpr_write_b32 a57, v248\n\t"
    "v_accvgpr_write_b32 a58, v248\n\t"
    "v_accvgpr_write_b32 a59, v248\n\t"
    "v_accvgpr_write_b32 a60, v248\n\t"
    "v_accvgpr_write_b32 a61, v248\n\t"
    "v_accvgpr_write_b32 a62, v248\n\t"
    "v_accvgpr_write_b32 a63, v248\n\t"
    "v_accvgpr_write_b32 a64, v248\n\t"
    "v_accvgpr_write_b32 a65, v248\n\t"
    "v_accvgpr_write_b32 a66, v248\n\t"
    "v_accvgpr_write_b32 a67, v248\n\t"
    "v_accvgpr_write_b32 a68, v248\n\t"
    "v_accvgpr_write_b32 a69, v248\n\t"
    "v_accvgpr_write_b32 a70, v248\n\t"
    "v_accvgpr_write_b32 a71, v248\n\t"
    "v_accvgpr_write_b32 a72, v248\n\t"
    "v_accvgpr_write_b32 a73, v248\n\t"
    "v_accvgpr_write_b32 a74, v248\n\t"
    "v_accvgpr_write_b32 a75, v248\n\t"
    "v_accvgpr_write_b32 a76, v248\n\t"
    "v_accvgpr_write_b32 a77, v248\n\t"
    "v_accvgpr_write_b32 a78, v248\n\t"
    "v_accvgpr_write_b32 a79, v248\n\t"
    "v_accvgpr_write_b32 a80, v248\n\t"
    "v_accvgpr_write_b32 a81, v248\n\t"
    "v_accvgpr_write_b32 a82, v248\n\t"
    "v_accvgpr_write_b32 a83, v248\n\t"
    "v_accvgpr_write_b32 a84, v248\n\t"
    "v_accvgpr_write_b32 a85, v248\n\t"
    "v_accvgpr_write_b32 a86, v248\n\t"
    "v_accvgpr_write_b32 a87, v248\n\t"
    "v_accvgpr_write_b32 a88, v248\n\t"
    "v_accvgpr_write_b32 a89, v248\n\t"
    "v_accvgpr_write_b32 a90, v248\n\t"
    "v_accvgpr_write_b32 a91, v248\n\t"
    "v_accvgpr_write_b32 a92, v248\n\t"
    "v_accvgpr_write_b32 a93, v248\n\t"
    "v_accvgpr_write_b32 a94, v248\n\t"
    "v_accvgpr_write_b32 a95, v248\n\t"
    "v_accvgpr_write_b32 a96, v248\n\t"
    "v_accvgpr_write_b32 a97, v248\n\t"
    "v_accvgpr_write_b32 a98, v248\n\t"
    "v_accvgpr_write_b32 a99, v248\n\t"
    "v_accvgpr_write_b32 a100, v248\n\t"
    "v_accvgpr_write_b32 a101, v248\n\t"
    "v_accvgpr_write_b32 a102, v248\n\t"
    "v_accvgpr_write_b32 a103, v248\n\t"
    "v_accvgpr_write_b32 a104, v248\n\t"
    "v_accvgpr_write_b32 a105, v248\n\t"
    "v_accvgpr_write_b32 a106, v248\n\t"
    "v_accvgpr_write_b32 a107, v248\n\t"
    "v_accvgpr_write_b32 a108, v248\n\t"
    "v_accvgpr_write_b32 a109, v248\n\t"
    "v_accvgpr_write_b32 a110, v248\n\t"
    "v_accvgpr_write_b32 a111, v248\n\t"
    "v_accvgpr_write_b32 a112, v248\n\t"
    "v_accvgpr_write_b32 a113, v248\n\t"
    "v_accvgpr_write_b32 a114, v248\n\t"
    "v_accvgpr_write_b32 a115, v248\n\t"
    "v_accvgpr_write_b32 a116, v248\n\t"
    "v_accvgpr_write_b32 a117, v248\n\t"
    "v_accvgpr_write_b32 a118, v248\n\t"
    "v_accvgpr_write_b32 a119, v248\n\t"
    "v_accvgpr_write_b32 a120, v248\n\t"
    "v_accvgpr_write_b32 a121, v248\n\t"
    "v_accvgpr_write_b32 a122, v248\n\t"
    "v_accvgpr_write_b32 a123, v248\n\t"
    "v_accvgpr_write_b32 a124, v248\n\t"
    "v_accvgpr_write_b32 a125, v248\n\t"
    "v_accvgpr_write_b32 a126, v248\n\t"
    "v_accvgpr_write_b32 a127, v248\n\t"
    "v_mov_b32 v244, 0\n\t"
    "v_mov_b32 v245, 0\n\t"
    "v_mov_b32 v246, 0\n\t"
    "v_mov_b32 v247, 0\n\t"
    "s_waitcnt vmcnt(28)\n\t"
    "v_lshlrev_b32 v248, 16, v0\n\t"
    "v_and_b32 v249, 0xffff0000, v0\n\t"
    "v_mul_f32 v248, 0x3e0293ee, v248\n\t"
    "v_mul_f32 v249, 0x3e0293ee, v249\n\t"
    "v_cvt_pk_bf16_f32 v248, v248, v249\n\t"
    "v_accvgpr_write_b32 a128, v248\n\t"
    "v_lshlrev_b32 v250, 16, v1\n\t"
    "v_and_b32 v251, 0xffff0000, v1\n\t"
    "v_mul_f32 v250, 0x3e0293ee, v250\n\t"
    "v_mul_f32 v251, 0x3e0293ee, v251\n\t"
    "v_cvt_pk_bf16_f32 v250, v250, v251\n\t"
    "v_accvgpr_write_b32 a129, v250\n\t"
    "v_lshlrev_b32 v248, 16, v2\n\t"
    "v_and_b32 v249, 0xffff0000, v2\n\t"
    "v_mul_f32 v248, 0x3e0293ee, v248\n\t"
    "v_mul_f32 v249, 0x3e0293ee, v249\n\t"
    "v_cvt_pk_bf16_f32 v248, v248, v249\n\t"
    "v_accvgpr_write_b32 a130, v248\n\t"
    "v_lshlrev_b32 v250, 16, v3\n\t"
    "v_and_b32 v251, 0xffff0000, v3\n\t"
    "v_mul_f32 v250, 0x3e0293ee, v250\n\t"
    "v_mul_f32 v251, 0x3e0293ee, v251\n\t"
    "v_cvt_pk_bf16_f32 v250, v250, v251\n\t"
    "v_accvgpr_write_b32 a131, v250\n\t"
    "v_lshlrev_b32 v248, 16, v4\n\t"
    "v_and_b32 v249, 0xffff0000, v4\n\t"
    "v_mul_f32 v248, 0x3e0293ee, v248\n\t"
    "v_mul_f32 v249, 0x3e0293ee, v249\n\t"
    "v_cvt_pk_bf16_f32 v248, v248, v249\n\t"
    "v_accvgpr_write_b32 a132, v248\n\t"
    "v_lshlrev_b32 v250, 16, v5\n\t"
    "v_and_b32 v251, 0xffff0000, v5\n\t"
    "v_mul_f32 v250, 0x3e0293ee, v250\n\t"
    "v_mul_f32 v251, 0x3e0293ee, v251\n\t"
    "v_cvt_pk_bf16_f32 v250, v250, v251\n\t"
    "v_accvgpr_write_b32 a133, v250\n\t"
    "v_lshlrev_b32 v248, 16, v6\n\t"
    "v_and_b32 v249, 0xffff0000, v6\n\t"
    "v_mul_f32 v248, 0x3e0293ee, v248\n\t"
    "v_mul_f32 v249, 0x3e0293ee, v249\n\t"
    "v_cvt_pk_bf16_f32 v248, v248, v249\n\t"
    "v_accvgpr_write_b32 a134, v248\n\t"
    "v_lshlrev_b32 v250, 16, v7\n\t"
    "v_and_b32 v251, 0xffff0000, v7\n\t"
    "v_mul_f32 v250, 0x3e0293ee, v250\n\t"
    "v_mul_f32 v251, 0x3e0293ee, v251\n\t"
    "v_cvt_pk_bf16_f32 v250, v250, v251\n\t"
    "v_accvgpr_write_b32 a135, v250\n\t"
    "v_lshlrev_b32 v248, 16, v8\n\t"
    "v_and_b32 v249, 0xffff0000, v8\n\t"
    "v_mul_f32 v248, 0x3e0293ee, v248\n\t"
    "v_mul_f32 v249, 0x3e0293ee, v249\n\t"
    "v_cvt_pk_bf16_f32 v248, v248, v249\n\t"
    "v_accvgpr_write_b32 a136, v248\n\t"
    "v_lshlrev_b32 v250, 16, v9\n\t"
    "v_and_b32 v251, 0xffff0000, v9\n\t"
    "v_mul_f32 v250, 0x3e0293ee, v250\n\t"
    "v_mul_f32 v251, 0x3e0293ee, v251\n\t"
    "v_cvt_pk_bf16_f32 v250, v250, v251\n\t"
    "v_accvgpr_write_b32 a137, v250\n\t"
    "v_lshlrev_b32 v248, 16, v10\n\t"
    "v_and_b32 v249, 0xffff0000, v10\n\t"
    "v_mul_f32 v248, 0x3e0293ee, v248\n\t"
    "v_mul_f32 v249, 0x3e0293ee, v249\n\t"
    "v_cvt_pk_bf16_f32 v248, v248, v249\n\t"
    "v_accvgpr_write_b32 a138, v248\n\t"
    "v_lshlrev_b32 v250, 16, v11\n\t"
    "v_and_b32 v251, 0xffff0000, v11\n\t"
    "v_mul_f32 v250, 0x3e0293ee, v250\n\t"
    "v_mul_f32 v251, 0x3e0293ee, v251\n\t"
    "v_cvt_pk_bf16_f32 v250, v250, v251\n\t"
    "v_accvgpr_write_b32 a139, v250\n\t"
    "v_lshlrev_b32 v248, 16, v12\n\t"
    "v_and_b32 v249, 0xffff0000, v12\n\t"
    "v_mul_f32 v248, 0x3e0293ee, v248\n\t"
    "v_mul_f32 v249, 0x3e0293ee, v249\n\t"
    "v_cvt_pk_bf16_f32 v248, v248, v249\n\t"
    "v_accvgpr_write_b32 a140, v248\n\t"
    "v_lshlrev_b32 v250, 16, v13\n\t"
    "v_and_b32 v251, 0xffff0000, v13\n\t"
    "v_mul_f32 v250, 0x3e0293ee, v250\n\t"
    "v_mul_f32 v251, 0x3e0293ee, v251\n\t"
    "v_cvt_pk_bf16_f32 v250, v250, v251\n\t"
    "v_accvgpr_write_b32 a141, v250\n\t"
    "v_lshlrev_b32 v248, 16, v14\n\t"
    "v_and_b32 v249, 0xffff0000, v14\n\t"
    "v_mul_f32 v248, 0x3e0293ee, v248\n\t"
    "v_mul_f32 v249, 0x3e0293ee, v249\n\t"
    "v_cvt_pk_bf16_f32 v248, v248, v249\n\t"
    "v_accvgpr_write_b32 a142, v248\n\t"
    "v_lshlrev_b32 v250, 16, v15\n\t"
    "v_and_b32 v251, 0xffff0000, v15\n\t"
    "v_mul_f32 v250, 0x3e0293ee, v250\n\t"
    "v_mul_f32 v251, 0x3e0293ee, v251\n\t"
    "v_cvt_pk_bf16_f32 v250, v250, v251\n\t"
    "v_accvgpr_write_b32 a143, v250\n\t"
    "v_lshlrev_b32 v248, 16, v16\n\t"
    "v_and_b32 v249, 0xffff0000, v16\n\t"
    "v_mul_f32 v248, 0x3e0293ee, v248\n\t"
    "v_mul_f32 v249, 0x3e0293ee, v249\n\t"
    "v_cvt_pk_bf16_f32 v248, v248, v249\n\t"
    "v_accvgpr_write_b32 a144, v248\n\t"
    "v_lshlrev_b32 v250, 16, v17\n\t"
    "v_and_b32 v251, 0xffff0000, v17\n\t"
    "v_mul_f32 v250, 0x3e0293ee, v250\n\t"
    "v_mul_f32 v251, 0x3e0293ee, v251\n\t"
    "v_cvt_pk_bf16_f32 v250, v250, v251\n\t"
    "v_accvgpr_write_b32 a145, v250\n\t"
    "v_lshlrev_b32 v248, 16, v18\n\t"
    "v_and_b32 v249, 0xffff0000, v18\n\t"
    "v_mul_f32 v248, 0x3e0293ee, v248\n\t"
    "v_mul_f32 v249, 0x3e0293ee, v249\n\t"
    "v_cvt_pk_bf16_f32 v248, v248, v249\n\t"
    "v_accvgpr_write_b32 a146, v248\n\t"
    "v_lshlrev_b32 v250, 16, v19\n\t"
    "v_and_b32 v251, 0xffff0000, v19\n\t"
    "v_mul_f32 v250, 0x3e0293ee, v250\n\t"
    "v_mul_f32 v251, 0x3e0293ee, v251\n\t"
    "v_cvt_pk_bf16_f32 v250, v250, v251\n\t"
    "v_accvgpr_write_b32 a147, v250\n\t"
    "v_lshlrev_b32 v248, 16, v20\n\t"
    "v_and_b32 v249, 0xffff0000, v20\n\t"
    "v_mul_f32 v248, 0x3e0293ee, v248\n\t"
    "v_mul_f32 v249, 0x3e0293ee, v249\n\t"
    "v_cvt_pk_bf16_f32 v248, v248, v249\n\t"
    "v_accvgpr_write_b32 a148, v248\n\t"
    "v_lshlrev_b32 v250, 16, v21\n\t"
    "v_and_b32 v251, 0xffff0000, v21\n\t"
    "v_mul_f32 v250, 0x3e0293ee, v250\n\t"
    "v_mul_f32 v251, 0x3e0293ee, v251\n\t"
    "v_cvt_pk_bf16_f32 v250, v250, v251\n\t"
    "v_accvgpr_write_b32 a149, v250\n\t"
    "v_lshlrev_b32 v248, 16, v22\n\t"
    "v_and_b32 v249, 0xffff0000, v22\n\t"
    "v_mul_f32 v248, 0x3e0293ee, v248\n\t"
    "v_mul_f32 v249, 0x3e0293ee, v249\n\t"
    "v_cvt_pk_bf16_f32 v248, v248, v249\n\t"
    "v_accvgpr_write_b32 a150, v248\n\t"
    "v_lshlrev_b32 v250, 16, v23\n\t"
    "v_and_b32 v251, 0xffff0000, v23\n\t"
    "v_mul_f32 v250, 0x3e0293ee, v250\n\t"
    "v_mul_f32 v251, 0x3e0293ee, v251\n\t"
    "v_cvt_pk_bf16_f32 v250, v250, v251\n\t"
    "v_accvgpr_write_b32 a151, v250\n\t"
    "v_lshlrev_b32 v248, 16, v24\n\t"
    "v_and_b32 v249, 0xffff0000, v24\n\t"
    "v_mul_f32 v248, 0x3e0293ee, v248\n\t"
    "v_mul_f32 v249, 0x3e0293ee, v249\n\t"
    "v_cvt_pk_bf16_f32 v248, v248, v249\n\t"
    "v_accvgpr_write_b32 a152, v248\n\t"
    "v_lshlrev_b32 v250, 16, v25\n\t"
    "v_and_b32 v251, 0xffff0000, v25\n\t"
    "v_mul_f32 v250, 0x3e0293ee, v250\n\t"
    "v_mul_f32 v251, 0x3e0293ee, v251\n\t"
    "v_cvt_pk_bf16_f32 v250, v250, v251\n\t"
    "v_accvgpr_write_b32 a153, v250\n\t"
    "v_lshlrev_b32 v248, 16, v26\n\t"
    "v_and_b32 v249, 0xffff0000, v26\n\t"
    "v_mul_f32 v248, 0x3e0293ee, v248\n\t"
    "v_mul_f32 v249, 0x3e0293ee, v249\n\t"
    "v_cvt_pk_bf16_f32 v248, v248, v249\n\t"
    "v_accvgpr_write_b32 a154, v248\n\t"
    "v_lshlrev_b32 v250, 16, v27\n\t"
    "v_and_b32 v251, 0xffff0000, v27\n\t"
    "v_mul_f32 v250, 0x3e0293ee, v250\n\t"
    "v_mul_f32 v251, 0x3e0293ee, v251\n\t"
    "v_cvt_pk_bf16_f32 v250, v250, v251\n\t"
    "v_accvgpr_write_b32 a155, v250\n\t"
    "v_lshlrev_b32 v248, 16, v28\n\t"
    "v_and_b32 v249, 0xffff0000, v28\n\t"
    "v_mul_f32 v248, 0x3e0293ee, v248\n\t"
    "v_mul_f32 v249, 0x3e0293ee, v249\n\t"
    "v_cvt_pk_bf16_f32 v248, v248, v249\n\t"
    "v_accvgpr_write_b32 a156, v248\n\t"
    "v_lshlrev_b32 v250, 16, v29\n\t"
    "v_and_b32 v251, 0xffff0000, v29\n\t"
    "v_mul_f32 v250, 0x3e0293ee, v250\n\t"
    "v_mul_f32 v251, 0x3e0293ee, v251\n\t"
    "v_cvt_pk_bf16_f32 v250, v250, v251\n\t"
    "v_accvgpr_write_b32 a157, v250\n\t"
    "v_lshlrev_b32 v248, 16, v30\n\t"
    "v_and_b32 v249, 0xffff0000, v30\n\t"
    "v_mul_f32 v248, 0x3e0293ee, v248\n\t"
    "v_mul_f32 v249, 0x3e0293ee, v249\n\t"
    "v_cvt_pk_bf16_f32 v248, v248, v249\n\t"
    "v_accvgpr_write_b32 a158, v248\n\t"
    "v_lshlrev_b32 v250, 16, v31\n\t"
    "v_and_b32 v251, 0xffff0000, v31\n\t"
    "v_mul_f32 v250, 0x3e0293ee, v250\n\t"
    "v_mul_f32 v251, 0x3e0293ee, v251\n\t"
    "v_cvt_pk_bf16_f32 v250, v250, v251\n\t"
    "v_accvgpr_write_b32 a159, v250\n\t"
    "v_lshlrev_b32 v248, 16, v32\n\t"
    "v_and_b32 v249, 0xffff0000, v32\n\t"
    "v_mul_f32 v248, 0x3e0293ee, v248\n\t"
    "v_mul_f32 v249, 0x3e0293ee, v249\n\t"
    "v_cvt_pk_bf16_f32 v248, v248, v249\n\t"
    "v_accvgpr_write_b32 a160, v248\n\t"
    "v_lshlrev_b32 v250, 16, v33\n\t"
    "v_and_b32 v251, 0xffff0000, v33\n\t"
    "v_mul_f32 v250, 0x3e0293ee, v250\n\t"
    "v_mul_f32 v251, 0x3e0293ee, v251\n\t"
    "v_cvt_pk_bf16_f32 v250, v250, v251\n\t"
    "v_accvgpr_write_b32 a161, v250\n\t"
    "v_lshlrev_b32 v248, 16, v34\n\t"
    "v_and_b32 v249, 0xffff0000, v34\n\t"
    "v_mul_f32 v248, 0x3e0293ee, v248\n\t"
    "v_mul_f32 v249, 0x3e0293ee, v249\n\t"
    "v_cvt_pk_bf16_f32 v248, v248, v249\n\t"
    "v_accvgpr_write_b32 a162, v248\n\t"
    "v_lshlrev_b32 v250, 16, v35\n\t"
    "v_and_b32 v251, 0xffff0000, v35\n\t"
    "v_mul_f32 v250, 0x3e0293ee, v250\n\t"
    "v_mul_f32 v251, 0x3e0293ee, v251\n\t"
    "v_cvt_pk_bf16_f32 v250, v250, v251\n\t"
    "v_accvgpr_write_b32 a163, v250\n\t"
    "v_lshlrev_b32 v248, 16, v36\n\t"
    "v_and_b32 v249, 0xffff0000, v36\n\t"
    "v_mul_f32 v248, 0x3e0293ee, v248\n\t"
    "v_mul_f32 v249, 0x3e0293ee, v249\n\t"
    "v_cvt_pk_bf16_f32 v248, v248, v249\n\t"
    "v_accvgpr_write_b32 a164, v248\n\t"
    "v_lshlrev_b32 v250, 16, v37\n\t"
    "v_and_b32 v251, 0xffff0000, v37\n\t"
    "v_mul_f32 v250, 0x3e0293ee, v250\n\t"
    "v_mul_f32 v251, 0x3e0293ee, v251\n\t"
    "v_cvt_pk_bf16_f32 v250, v250, v251\n\t"
    "v_accvgpr_write_b32 a165, v250\n\t"
    "v_lshlrev_b32 v248, 16, v38\n\t"
    "v_and_b32 v249, 0xffff0000, v38\n\t"
    "v_mul_f32 v248, 0x3e0293ee, v248\n\t"
    "v_mul_f32 v249, 0x3e0293ee, v249\n\t"
    "v_cvt_pk_bf16_f32 v248, v248, v249\n\t"
    "v_accvgpr_write_b32 a166, v248\n\t"
    "v_lshlrev_b32 v250, 16, v39\n\t"
    "v_and_b32 v251, 0xffff0000, v39\n\t"
    "v_mul_f32 v250, 0x3e0293ee, v250\n\t"
    "v_mul_f32 v251, 0x3e0293ee, v251\n\t"
    "v_cvt_pk_bf16_f32 v250, v250, v251\n\t"
    "v_accvgpr_write_b32 a167, v250\n\t"
    "v_lshlrev_b32 v248, 16, v40\n\t"
    "v_and_b32 v249, 0xffff0000, v40\n\t"
    "v_mul_f32 v248, 0x3e0293ee, v248\n\t"
    "v_mul_f32 v249, 0x3e0293ee, v249\n\t"
    "v_cvt_pk_bf16_f32 v248, v248, v249\n\t"
    "v_accvgpr_write_b32 a168, v248\n\t"
    "v_lshlrev_b32 v250, 16, v41\n\t"
    "v_and_b32 v251, 0xffff0000, v41\n\t"
    "v_mul_f32 v250, 0x3e0293ee, v250\n\t"
    "v_mul_f32 v251, 0x3e0293ee, v251\n\t"
    "v_cvt_pk_bf16_f32 v250, v250, v251\n\t"
    "v_accvgpr_write_b32 a169, v250\n\t"
    "v_lshlrev_b32 v248, 16, v42\n\t"
    "v_and_b32 v249, 0xffff0000, v42\n\t"
    "v_mul_f32 v248, 0x3e0293ee, v248\n\t"
    "v_mul_f32 v249, 0x3e0293ee, v249\n\t"
    "v_cvt_pk_bf16_f32 v248, v248, v249\n\t"
    "v_accvgpr_write_b32 a170, v248\n\t"
    "v_lshlrev_b32 v250, 16, v43\n\t"
    "v_and_b32 v251, 0xffff0000, v43\n\t"
    "v_mul_f32 v250, 0x3e0293ee, v250\n\t"
    "v_mul_f32 v251, 0x3e0293ee, v251\n\t"
    "v_cvt_pk_bf16_f32 v250, v250, v251\n\t"
    "v_accvgpr_write_b32 a171, v250\n\t"
    "v_lshlrev_b32 v248, 16, v44\n\t"
    "v_and_b32 v249, 0xffff0000, v44\n\t"
    "v_mul_f32 v248, 0x3e0293ee, v248\n\t"
    "v_mul_f32 v249, 0x3e0293ee, v249\n\t"
    "v_cvt_pk_bf16_f32 v248, v248, v249\n\t"
    "v_accvgpr_write_b32 a172, v248\n\t"
    "v_lshlrev_b32 v250, 16, v45\n\t"
    "v_and_b32 v251, 0xffff0000, v45\n\t"
    "v_mul_f32 v250, 0x3e0293ee, v250\n\t"
    "v_mul_f32 v251, 0x3e0293ee, v251\n\t"
    "v_cvt_pk_bf16_f32 v250, v250, v251\n\t"
    "v_accvgpr_write_b32 a173, v250\n\t"
    "v_lshlrev_b32 v248, 16, v46\n\t"
    "v_and_b32 v249, 0xffff0000, v46\n\t"
    "v_mul_f32 v248, 0x3e0293ee, v248\n\t"
    "v_mul_f32 v249, 0x3e0293ee, v249\n\t"
    "v_cvt_pk_bf16_f32 v248, v248, v249\n\t"
    "v_accvgpr_write_b32 a174, v248\n\t"
    "v_lshlrev_b32 v250, 16, v47\n\t"
    "v_and_b32 v251, 0xffff0000, v47\n\t"
    "v_mul_f32 v250, 0x3e0293ee, v250\n\t"
    "v_mul_f32 v251, 0x3e0293ee, v251\n\t"
    "v_cvt_pk_bf16_f32 v250, v250, v251\n\t"
    "v_accvgpr_write_b32 a175, v250\n\t"
    "v_lshlrev_b32 v248, 16, v48\n\t"
    "v_and_b32 v249, 0xffff0000, v48\n\t"
    "v_mul_f32 v248, 0x3e0293ee, v248\n\t"
    "v_mul_f32 v249, 0x3e0293ee, v249\n\t"
    "v_cvt_pk_bf16_f32 v248, v248, v249\n\t"
    "v_accvgpr_write_b32 a176, v248\n\t"
    "v_lshlrev_b32 v250, 16, v49\n\t"
    "v_and_b32 v251, 0xffff0000, v49\n\t"
    "v_mul_f32 v250, 0x3e0293ee, v250\n\t"
    "v_mul_f32 v251, 0x3e0293ee, v251\n\t"
    "v_cvt_pk_bf16_f32 v250, v250, v251\n\t"
    "v_accvgpr_write_b32 a177, v250\n\t"
    "v_lshlrev_b32 v248, 16, v50\n\t"
    "v_and_b32 v249, 0xffff0000, v50\n\t"
    "v_mul_f32 v248, 0x3e0293ee, v248\n\t"
    "v_mul_f32 v249, 0x3e0293ee, v249\n\t"
    "v_cvt_pk_bf16_f32 v248, v248, v249\n\t"
    "v_accvgpr_write_b32 a178, v248\n\t"
    "v_lshlrev_b32 v250, 16, v51\n\t"
    "v_and_b32 v251, 0xffff0000, v51\n\t"
    "v_mul_f32 v250, 0x3e0293ee, v250\n\t"
    "v_mul_f32 v251, 0x3e0293ee, v251\n\t"
    "v_cvt_pk_bf16_f32 v250, v250, v251\n\t"
    "v_accvgpr_write_b32 a179, v250\n\t"
    "v_lshlrev_b32 v248, 16, v52\n\t"
    "v_and_b32 v249, 0xffff0000, v52\n\t"
    "v_mul_f32 v248, 0x3e0293ee, v248\n\t"
    "v_mul_f32 v249, 0x3e0293ee, v249\n\t"
    "v_cvt_pk_bf16_f32 v248, v248, v249\n\t"
    "v_accvgpr_write_b32 a180, v248\n\t"
    "v_lshlrev_b32 v250, 16, v53\n\t"
    "v_and_b32 v251, 0xffff0000, v53\n\t"
    "v_mul_f32 v250, 0x3e0293ee, v250\n\t"
    "v_mul_f32 v251, 0x3e0293ee, v251\n\t"
    "v_cvt_pk_bf16_f32 v250, v250, v251\n\t"
    "v_accvgpr_write_b32 a181, v250\n\t"
    "v_lshlrev_b32 v248, 16, v54\n\t"
    "v_and_b32 v249, 0xffff0000, v54\n\t"
    "v_mul_f32 v248, 0x3e0293ee, v248\n\t"
    "v_mul_f32 v249, 0x3e0293ee, v249\n\t"
    "v_cvt_pk_bf16_f32 v248, v248, v249\n\t"
    "v_accvgpr_write_b32 a182, v248\n\t"
    "v_lshlrev_b32 v250, 16, v55\n\t"
    "v_and_b32 v251, 0xffff0000, v55\n\t"
    "v_mul_f32 v250, 0x3e0293ee, v250\n\t"
    "v_mul_f32 v251, 0x3e0293ee, v251\n\t"
    "v_cvt_pk_bf16_f32 v250, v250, v251\n\t"
    "v_accvgpr_write_b32 a183, v250\n\t"
    "v_lshlrev_b32 v248, 16, v56\n\t"
    "v_and_b32 v249, 0xffff0000, v56\n\t"
    "v_mul_f32 v248, 0x3e0293ee, v248\n\t"
    "v_mul_f32 v249, 0x3e0293ee, v249\n\t"
    "v_cvt_pk_bf16_f32 v248, v248, v249\n\t"
    "v_accvgpr_write_b32 a184, v248\n\t"
    "v_lshlrev_b32 v250, 16, v57\n\t"
    "v_and_b32 v251, 0xffff0000, v57\n\t"
    "v_mul_f32 v250, 0x3e0293ee, v250\n\t"
    "v_mul_f32 v251, 0x3e0293ee, v251\n\t"
    "v_cvt_pk_bf16_f32 v250, v250, v251\n\t"
    "v_accvgpr_write_b32 a185, v250\n\t"
    "v_lshlrev_b32 v248, 16, v58\n\t"
    "v_and_b32 v249, 0xffff0000, v58\n\t"
    "v_mul_f32 v248, 0x3e0293ee, v248\n\t"
    "v_mul_f32 v249, 0x3e0293ee, v249\n\t"
    "v_cvt_pk_bf16_f32 v248, v248, v249\n\t"
    "v_accvgpr_write_b32 a186, v248\n\t"
    "v_lshlrev_b32 v250, 16, v59\n\t"
    "v_and_b32 v251, 0xffff0000, v59\n\t"
    "v_mul_f32 v250, 0x3e0293ee, v250\n\t"
    "v_mul_f32 v251, 0x3e0293ee, v251\n\t"
    "v_cvt_pk_bf16_f32 v250, v250, v251\n\t"
    "v_accvgpr_write_b32 a187, v250\n\t"
    "v_lshlrev_b32 v248, 16, v60\n\t"
    "v_and_b32 v249, 0xffff0000, v60\n\t"
    "v_mul_f32 v248, 0x3e0293ee, v248\n\t"
    "v_mul_f32 v249, 0x3e0293ee, v249\n\t"
    "v_cvt_pk_bf16_f32 v248, v248, v249\n\t"
    "v_accvgpr_write_b32 a188, v248\n\t"
    "v_lshlrev_b32 v250, 16, v61\n\t"
    "v_and_b32 v251, 0xffff0000, v61\n\t"
    "v_mul_f32 v250, 0x3e0293ee, v250\n\t"
    "v_mul_f32 v251, 0x3e0293ee, v251\n\t"
    "v_cvt_pk_bf16_f32 v250, v250, v251\n\t"
    "v_accvgpr_write_b32 a189, v250\n\t"
    "v_lshlrev_b32 v248, 16, v62\n\t"
    "v_and_b32 v249, 0xffff0000, v62\n\t"
    "v_mul_f32 v248, 0x3e0293ee, v248\n\t"
    "v_mul_f32 v249, 0x3e0293ee, v249\n\t"
    "v_cvt_pk_bf16_f32 v248, v248, v249\n\t"
    "v_accvgpr_write_b32 a190, v248\n\t"
    "v_lshlrev_b32 v250, 16, v63\n\t"
    "v_and_b32 v251, 0xffff0000, v63\n\t"
    "v_mul_f32 v250, 0x3e0293ee, v250\n\t"
    "v_mul_f32 v251, 0x3e0293ee, v251\n\t"
    "v_cvt_pk_bf16_f32 v250, v250, v251\n\t"
    "v_accvgpr_write_b32 a191, v250\n\t"
    "s_waitcnt vmcnt(24)\n\t"
    "s_barrier\n\t"
    "ds_read_b128 a[192:195], v224 offset:0\n\t"
    "ds_read_b128 a[196:199], v225 offset:0\n\t"
    "ds_read_b128 a[200:203], v226 offset:0\n\t"
    "ds_read_b128 a[204:207], v227 offset:0\n\t"
    "ds_read_b128 a[208:211], v228 offset:0\n\t"
    "ds_read_b128 a[212:215], v229 offset:0\n\t"
    "ds_read_b128 a[216:219], v230 offset:0\n\t"
    "ds_read_b128 a[220:223], v231 offset:0\n\t"
    "ds_read_b128 a[224:227], v224 offset:8192\n\t"
    "ds_read_b128 a[228:231], v225 offset:8192\n\t"
    "ds_read_b128 a[232:235], v226 offset:8192\n\t"
    "ds_read_b128 a[236:239], v227 offset:8192\n\t"
    "ds_read_b128 a[240:243], v228 offset:8192\n\t"
    "ds_read_b128 a[244:247], v229 offset:8192\n\t"
    "ds_read_b128 a[248:251], v230 offset:8192\n\t"
    "ds_read_b128 a[252:255], v231 offset:8192\n\t"
    "s_waitcnt lgkmcnt(0)\n\t"
    "v_mfma_f32_32x32x16_bf16 v[0:15], a[192:195], a[128:131], 0\n\t"
    "v_mfma_f32_32x32x16_bf16 v[16:31], a[192:195], a[160:163], 0\n\t"
    "v_mfma_f32_32x32x16_bf16 v[32:47], a[224:227], a[128:131], 0\n\t"
    "v_mfma_f32_32x32x16_bf16 v[48:63], a[224:227], a[160:163], 0\n\t"
    "v_mfma_f32_32x32x16_bf16 v[0:15], a[196:199], a[132:135], v[0:15]\n\t"
    "v_mfma_f32_32x32x16_bf16 v[16:31], a[196:199], a[164:167], v[16:31]\n\t"
    "v_mfma_f32_32x32x16_bf16 v[32:47], a[228:231], a[132:135], v[32:47]\n\t"
    "v_mfma_f32_32x32x16_bf16 v[48:63], a[228:231], a[164:167], v[48:63]\n\t"
    "v_mfma_f32_32x32x16_bf16 v[0:15], a[200:203], a[136:139], v[0:15]\n\t"
    "v_mfma_f32_32x32x16_bf16 v[16:31], a[200:203], a[168:171], v[16:31]\n\t"
    "v_mfma_f32_32x32x16_bf16 v[32:47], a[232:235], a[136:139], v[32:47]\n\t"
    "v_mfma_f32_32x32x16_bf16 v[48:63], a[232:235], a[168:171], v[48:63]\n\t"
    "v_mfma_f32_32x32x16_bf16 v[0:15], a[204:207], a[140:143], v[0:15]\n\t"
    "v_mfma_f32_32x32x16_bf16 v[16:31], a[204:207], a[172:175], v[16:31]\n\t"
    "v_mfma_f32_32x32x16_bf16 v[32:47], a[236:239], a[140:143], v[32:47]\n\t"
    "v_mfma_f32_32x32x16_bf16 v[48:63], a[236:239], a[172:175], v[48:63]\n\t"
    "v_mfma_f32_32x32x16_bf16 v[0:15], a[208:211], a[144:147], v[0:15]\n\t"
    "v_mfma_f32_32x32x16_bf16 v[16:31], a[208:211], a[176:179], v[16:31]\n\t"
    "v_mfma_f32_32x32x16_bf16 v[32:47], a[240:243], a[144:147], v[32:47]\n\t"
    "v_mfma_f32_32x32x16_bf16 v[48:63], a[240:243], a[176:179], v[48:63]\n\t"
    "v_mfma_f32_32x32x16_bf16 v[0:15], a[212:215], a[148:151], v[0:15]\n\t"
    "v_mfma_f32_32x32x16_bf16 v[16:31], a[212:215], a[180:183], v[16:31]\n\t"
    "v_mfma_f32_32x32x16_bf16 v[32:47], a[244:247], a[148:151], v[32:47]\n\t"
    "v_mfma_f32_32x32x16_bf16 v[48:63], a[244:247], a[180:183], v[48:63]\n\t"
    "v_mfma_f32_32x32x16_bf16 v[0:15], a[216:219], a[152:155], v[0:15]\n\t"
    "v_mfma_f32_32x32x16_bf16 v[16:31], a[216:219], a[184:187], v[16:31]\n\t"
    "v_mfma_f32_32x32x16_bf16 v[32:47], a[248:251], a[152:155], v[32:47]\n\t"
    "v_mfma_f32_32x32x16_bf16 v[48:63], a[248:251], a[184:187], v[48:63]\n\t"
    "v_mfma_f32_32x32x16_bf16 v[0:15], a[220:223], a[156:159], v[0:15]\n\t"
    "v_mfma_f32_32x32x16_bf16 v[16:31], a[220:223], a[188:191], v[16:31]\n\t"
    "v_mfma_f32_32x32x16_bf16 v[32:47], a[252:255], a[156:159], v[32:47]\n\t"
    "v_mfma_f32_32x32x16_bf16 v[48:63], a[252:255], a[188:191], v[48:63]\n\t"
    "s_waitcnt vmcnt(16) lgkmcnt(0)\n\t"
    "s_barrier\n\t"
    "ds_read_b128 a[192:195], v224 offset:16384\n\t"
    "ds_read_b128 a[196:199], v225 offset:16384\n\t"
    "ds_read_b128 a[200:203], v226 offset:16384\n\t"
    "ds_read_b128 a[204:207], v227 offset:16384\n\t"
    "ds_read_b128 a[208:211], v228 offset:16384\n\t"
    "ds_read_b128 a[212:215], v229 offset:16384\n\t"
    "ds_read_b128 a[216:219], v230 offset:16384\n\t"
    "ds_read_b128 a[220:223], v231 offset:16384\n\t"
    "ds_read_b128 a[224:227], v224 offset:24576\n\t"
    "ds_read_b128 a[228:231], v225 offset:24576\n\t"
    "ds_read_b128 a[232:235], v226 offset:24576\n\t"
    "ds_read_b128 a[236:239], v227 offset:24576\n\t"
    "ds_read_b128 a[240:243], v228 offset:24576\n\t"
    "ds_read_b128 a[244:247], v229 offset:24576\n\t"
    "ds_read_b128 a[248:251], v230 offset:24576\n\t"
    "ds_read_b128 a[252:255], v231 offset:24576\n\t"
    "v_max3_f32 v248, v0, v1, v2\n\t"
    "v_max3_f32 v248, v248, v3, v4\n\t"
    "v_max3_f32 v248, v248, v5, v6\n\t"
    "v_max3_f32 v248, v248, v7, v8\n\t"
    "v_max3_f32 v248, v248, v9, v10\n\t"
    "v_max3_f32 v248, v248, v11, v12\n\t"
    "v_max3_f32 v248, v248, v13, v14\n\t"
    "v_max3_f32 v248, v248, v15, v32\n\t"
    "v_max3_f32 v248, v248, v33, v34\n\t"
    "v_max3_f32 v248, v248, v35, v36\n\t"
    "v_max3_f32 v248, v248, v37, v38\n\t"
    "v_max3_f32 v248, v248, v39, v40\n\t"
    "v_max3_f32 v248, v248, v41, v42\n\t"
    "v_max3_f32 v248, v248, v43, v44\n\t"
    "v_max3_f32 v248, v248, v45, v46\n\t"
    "v_max_f32 v248, v248, v47\n\t"
    "v_max3_f32 v249, v16, v17, v18\n\t"
    "v_max3_f32 v249, v249, v19, v20\n\t"
    "v_max3_f32 v249, v249, v21, v22\n\t"
    "v_max3_f32 v249, v249, v23, v24\n\t"
    "v_max3_f32 v249, v249, v25, v26\n\t"
    "v_max3_f32 v249, v249, v27, v28\n\t"
    "v_max3_f32 v249, v249, v29, v30\n\t"
    "v_max3_f32 v249, v249, v31, v48\n\t"
    "v_max3_f32 v249, v249, v49, v50\n\t"
    "v_max3_f32 v249, v249, v51, v52\n\t"
    "v_max3_f32 v249, v249, v53, v54\n\t"
    "v_max3_f32 v249, v249, v55, v56\n\t"
    "v_max3_f32 v249, v249, v57, v58\n\t"
    "v_max3_f32 v249, v249, v59, v60\n\t"
    "v_max3_f32 v249, v249, v61, v62\n\t"
    "v_max_f32 v249, v249, v63\n\t"
    "v_mov_b32 v252, v248\n\t"
    "s_nop 1\n\t"
    "v_permlane32_swap_b32 v248, v252\n\t"
    "v_max_f32 v248, v248, v252\n\t"
    "v_mov_b32 v253, v248\n\t"
    "v_mov_b32 v242, v248\n\t"
    "v_sub_f32 v0, v0, v253\n\t"
    "v_sub_f32 v1, v1, v253\n\t"
    "v_sub_f32 v2, v2, v253\n\t"
    "v_sub_f32 v3, v3, v253\n\t"
    "v_sub_f32 v4, v4, v253\n\t"
    "v_sub_f32 v5, v5, v253\n\t"
    "v_sub_f32 v6, v6, v253\n\t"
    "v_sub_f32 v7, v7, v253\n\t"
    "v_sub_f32 v8, v8, v253\n\t"
    "v_sub_f32 v9, v9, v253\n\t"
    "v_sub_f32 v10, v10, v253\n\t"
    "v_sub_f32 v11, v11, v253\n\t"
    "v_sub_f32 v12, v12, v253\n\t"
    "v_sub_f32 v13, v13, v253\n\t"
    "v_sub_f32 v14, v14, v253\n\t"
    "v_sub_f32 v15, v15, v253\n\t"
    "v_sub_f32 v32, v32, v253\n\t"
    "v_sub_f32 v33, v33, v253\n\t"
    "v_sub_f32 v34, v34, v253\n\t"
    "v_sub_f32 v35, v35, v253\n\t"
    "v_sub_f32 v36, v36, v253\n\t"
    "v_sub_f32 v37, v37, v253\n\t"
    "v_sub_f32 v38, v38, v253\n\t"
    "v_sub_f32 v39, v39, v253\n\t"
    "v_sub_f32 v40, v40, v253\n\t"
    "v_sub_f32 v41, v41, v253\n\t"
    "v_sub_f32 v42, v42, v253\n\t"
    "v_sub_f32 v43, v43, v253\n\t"
    "v_sub_f32 v44, v44, v253\n\t"
    "v_sub_f32 v45, v45, v253\n\t"
    "v_sub_f32 v46, v46, v253\n\t"
    "v_sub_f32 v47, v47, v253\n\t"
    "v_sub_f32 v252, 0, v242\n\t"
    "v_mov_b32 v192, v252\n\t"
    "v_mov_b32 v193, v252\n\t"
    "v_mov_b32 v194, v252\n\t"
    "v_mov_b32 v195, v252\n\t"
    "v_mov_b32 v196, v252\n\t"
    "v_mov_b32 v197, v252\n\t"
    "v_mov_b32 v198, v252\n\t"
    "v_mov_b32 v199, v252\n\t"
    "v_mov_b32 v200, v252\n\t"
    "v_mov_b32 v201, v252\n\t"
    "v_mov_b32 v202, v252\n\t"
    "v_mov_b32 v203, v252\n\t"
    "v_mov_b32 v204, v252\n\t"
    "v_mov_b32 v205, v252\n\t"
    "v_mov_b32 v206, v252\n\t"
    "v_mov_b32 v207, v252\n\t"
    "v_mov_b32 v252, v249\n\t"
    "s_nop 1\n\t"
    "v_permlane32_swap_b32 v249, v252\n\t"
    "v_max_f32 v249, v249, v252\n\t"
    "v_mov_b32 v253, v249\n\t"
    "v_mov_b32 v243, v249\n\t"
    "v_sub_f32 v16, v16, v253\n\t"
    "v_sub_f32 v17, v17, v253\n\t"
    "v_sub_f32 v18, v18, v253\n\t"
    "v_sub_f32 v19, v19, v253\n\t"
    "v_sub_f32 v20, v20, v253\n\t"
    "v_sub_f32 v21, v21, v253\n\t"
    "v_sub_f32 v22, v22, v253\n\t"
    "v_sub_f32 v23, v23, v253\n\t"
    "v_sub_f32 v24, v24, v253\n\t"
    "v_sub_f32 v25, v25, v253\n\t"
    "v_sub_f32 v26, v26, v253\n\t"
    "v_sub_f32 v27, v27, v253\n\t"
    "v_sub_f32 v28, v28, v253\n\t"
    "v_sub_f32 v29, v29, v253\n\t"
    "v_sub_f32 v30, v30, v253\n\t"
    "v_sub_f32 v31, v31, v253\n\t"
    "v_sub_f32 v48, v48, v253\n\t"
    "v_sub_f32 v49, v49, v253\n\t"
    "v_sub_f32 v50, v50, v253\n\t"
    "v_sub_f32 v51, v51, v253\n\t"
    "v_sub_f32 v52, v52, v253\n\t"
    "v_sub_f32 v53, v53, v253\n\t"
    "v_sub_f32 v54, v54, v253\n\t"
    "v_sub_f32 v55, v55, v253\n\t"
    "v_sub_f32 v56, v56, v253\n\t"
    "v_sub_f32 v57, v57, v253\n\t"
    "v_sub_f32 v58, v58, v253\n\t"
    "v_sub_f32 v59, v59, v253\n\t"
    "v_sub_f32 v60, v60, v253\n\t"
    "v_sub_f32 v61, v61, v253\n\t"
    "v_sub_f32 v62, v62, v253\n\t"
    "v_sub_f32 v63, v63, v253\n\t"
    "v_sub_f32 v252, 0, v243\n\t"
    "v_mov_b32 v208, v252\n\t"
    "v_mov_b32 v209, v252\n\t"
    "v_mov_b32 v210, v252\n\t"
    "v_mov_b32 v211, v252\n\t"
    "v_mov_b32 v212, v252\n\t"
    "v_mov_b32 v213, v252\n\t"
    "v_mov_b32 v214, v252\n\t"
    "v_mov_b32 v215, v252\n\t"
    "v_mov_b32 v216, v252\n\t"
    "v_mov_b32 v217, v252\n\t"
    "v_mov_b32 v218, v252\n\t"
    "v_mov_b32 v219, v252\n\t"
    "v_mov_b32 v220, v252\n\t"
    "v_mov_b32 v221, v252\n\t"
    "v_mov_b32 v222, v252\n\t"
    "v_mov_b32 v223, v252\n\t"
    "v_exp_f32 v0, v0\n\t"
    "v_exp_f32 v1, v1\n\t"
    "v_exp_f32 v2, v2\n\t"
    "v_add_f32 v244, v244, v0\n\t"
    "v_exp_f32 v3, v3\n\t"
    "v_add_f32 v245, v245, v1\n\t"
    "v_exp_f32 v4, v4\n\t"
    "v_add_f32 v244, v244, v2\n\t"
    "v_exp_f32 v5, v5\n\t"
    "v_add_f32 v245, v245, v3\n\t"
    "v_exp_f32 v6, v6\n\t"
    "v_add_f32 v244, v244, v4\n\t"
    "v_exp_f32 v7, v7\n\t"
    "v_add_f32 v245, v245, v5\n\t"
    "v_exp_f32 v8, v8\n\t"
    "v_add_f32 v244, v244, v6\n\t"
    "v_exp_f32 v9, v9\n\t"
    "v_add_f32 v245, v245, v7\n\t"
    "v_exp_f32 v10, v10\n\t"
    "v_add_f32 v244, v244, v8\n\t"
    "v_exp_f32 v11, v11\n\t"
    "v_add_f32 v245, v245, v9\n\t"
    "v_exp_f32 v12, v12\n\t"
    "v_add_f32 v244, v244, v10\n\t"
    "v_exp_f32 v13, v13\n\t"
    "v_add_f32 v245, v245, v11\n\t"
    "v_exp_f32 v14, v14\n\t"
    "v_add_f32 v244, v244, v12\n\t"
    "v_exp_f32 v15, v15\n\t"
    "v_add_f32 v245, v245, v13\n\t"
    "v_exp_f32 v16, v16\n\t"
    "v_add_f32 v244, v244, v14\n\t"
    "v_exp_f32 v17, v17\n\t"
    "v_add_f32 v245, v245, v15\n\t"
    "v_exp_f32 v18, v18\n\t"
    "v_add_f32 v246, v246, v16\n\t"
    "v_exp_f32 v19, v19\n\t"
    "v_add_f32 v247, v247, v17\n\t"
    "v_exp_f32 v20, v20\n\t"
    "v_add_f32 v246, v246, v18\n\t"
    "v_exp_f32 v21, v21\n\t"
    "v_add_f32 v247, v247, v19\n\t"
    "v_exp_f32 v22, v22\n\t"
    "v_add_f32 v246, v246, v20\n\t"
    "v_exp_f32 v23, v23\n\t"
    "v_add_f32 v247, v247, v21\n\t"
    "v_exp_f32 v24, v24\n\t"
    "v_add_f32 v246, v246, v22\n\t"
    "v_exp_f32 v25, v25\n\t"
    "v_add_f32 v247, v247, v23\n\t"
    "v_exp_f32 v26, v26\n\t"
    "v_add_f32 v246, v246, v24\n\t"
    "v_exp_f32 v27, v27\n\t"
    "v_add_f32 v247, v247, v25\n\t"
    "v_exp_f32 v28, v28\n\t"
    "v_add_f32 v246, v246, v26\n\t"
    "v_exp_f32 v29, v29\n\t"
    "v_add_f32 v247, v247, v27\n\t"
    "v_exp_f32 v30, v30\n\t"
    "v_add_f32 v246, v246, v28\n\t"
    "v_exp_f32 v31, v31\n\t"
    "v_add_f32 v247, v247, v29\n\t"
    "v_exp_f32 v32, v32\n\t"
    "v_add_f32 v246, v246, v30\n\t"
    "v_exp_f32 v33, v33\n\t"
    "v_add_f32 v247, v247, v31\n\t"
    "v_exp_f32 v34, v34\n\t"
    "v_add_f32 v244, v244, v32\n\t"
    "v_exp_f32 v35, v35\n\t"
    "v_add_f32 v245, v245, v33\n\t"
    "v_exp_f32 v36, v36\n\t"
    "v_add_f32 v244, v244, v34\n\t"
    "v_exp_f32 v37, v37\n\t"
    "v_add_f32 v245, v245, v35\n\t"
    "v_exp_f32 v38, v38\n\t"
    "v_add_f32 v244, v244, v36\n\t"
    "v_exp_f32 v39, v39\n\t"
    "v_add_f32 v245, v245, v37\n\t"
    "v_add_f32 v244, v244, v38\n\t"
    "v_add_f32 v245, v245, v39\n\t"
    "s_waitcnt lgkmcnt(0)\n\t"
    "s_mov_b32 s43, 0\n\t"
    "LOOP%=:\n\t"
    "v_mfma_f32_32x32x16_bf16 v[64:79], a[192:195], a[128:131], v[192:207]\n\t"
    "ds_read_b64_tr_b16 v[128:129], v232 offset:0\n\t"
    "ds_read_b64_tr_b16 v[130:131], v232 offset:2048\n\t"
    "v_exp_f32 v40, v40\n\t"
    "v_cvt_pk_bf16_f32 v0, v0, v1\n\t"
    "v_mfma_f32_32x32x16_bf16 v[80:95], a[192:195], a[160:163], v[208:223]\n\t"
    "ds_read_b64_tr_b16 v[144:145], v233 offset:0\n\t"
    "ds_read_b64_tr_b16 v[146:147], v233 offset:2048\n\t"
    "v_exp_f32 v41, v41\n\t"
    "v_add_f32 v244, v244, v40\n\t"
    "v_cvt_pk_bf16_f32 v1, v2, v3\n\t"
    "v_mfma_f32_32x32x16_bf16 v[96:111], a[224:227], a[128:131], v[192:207]\n\t"
    "ds_read_b64_tr_b16 v[160:161], v234 offset:0\n\t"
    "ds_read_b64_tr_b16 v[162:163], v234 offset:2048\n\t"
    "v_exp_f32 v42, v42\n\t"
    "v_add_f32 v245, v245, v41\n\t"
    "v_cvt_pk_bf16_f32 v2, v4, v5\n\t"
    "v_mfma_f32_32x32x16_bf16 v[112:127], a[224:227], a[160:163], v[208:223]\n\t"
    "ds_read_b64_tr_b16 v[176:177], v235 offset:0\n\t"
    "ds_read_b64_tr_b16 v[178:179], v235 offset:2048\n\t"
    "v_exp_f32 v43, v43\n\t"
    "v_add_f32 v244, v244, v42\n\t"
    "v_cvt_pk_bf16_f32 v3, v6, v7\n\t"
    "v_mfma_f32_32x32x16_bf16 v[64:79], a[196:199], a[132:135], v[64:79]\n\t"
    "ds_read_b64_tr_b16 v[132:133], v232 offset:4096\n\t"
    "ds_read_b64_tr_b16 v[134:135], v232 offset:6144\n\t"
    "v_exp_f32 v44, v44\n\t"
    "v_add_f32 v245, v245, v43\n\t"
    "v_cvt_pk_bf16_f32 v16, v16, v17\n\t"
    "v_mfma_f32_32x32x16_bf16 v[80:95], a[196:199], a[164:167], v[80:95]\n\t"
    "ds_read_b64_tr_b16 v[148:149], v233 offset:4096\n\t"
    "ds_read_b64_tr_b16 v[150:151], v233 offset:6144\n\t"
    "v_exp_f32 v45, v45\n\t"
    "v_add_f32 v244, v244, v44\n\t"
    "v_cvt_pk_bf16_f32 v17, v18, v19\n\t"
    "v_mfma_f32_32x32x16_bf16 v[96:111], a[228:231], a[132:135], v[96:111]\n\t"
    "ds_read_b64_tr_b16 v[164:165], v234 offset:4096\n\t"
    "ds_read_b64_tr_b16 v[166:167], v234 offset:6144\n\t"
    "v_exp_f32 v46, v46\n\t"
    "v_add_f32 v245, v245, v45\n\t"
    "v_cvt_pk_bf16_f32 v18, v20, v21\n\t"
    "v_mfma_f32_32x32x16_bf16 v[112:127], a[228:231], a[164:167], v[112:127]\n\t"
    "ds_read_b64_tr_b16 v[180:181], v235 offset:4096\n\t"
    "ds_read_b64_tr_b16 v[182:183], v235 offset:6144\n\t"
    "v_exp_f32 v47, v47\n\t"
    "v_add_f32 v244, v244, v46\n\t"
    "v_cvt_pk_bf16_f32 v19, v22, v23\n\t"
    "v_mfma_f32_32x32x16_bf16 v[64:79], a[200:203], a[136:139], v[64:79]\n\t"
    "ds_read_b64_tr_b16 v[136:137], v232 offset:8192\n\t"
    "ds_read_b64_tr_b16 v[138:139], v232 offset:10240\n\t"
    "v_exp_f32 v48, v48\n\t"
    "v_add_f32 v245, v245, v47\n\t"
    "v_cvt_pk_bf16_f32 v8, v8, v9\n\t"
    "v_mfma_f32_32x32x16_bf16 v[80:95], a[200:203], a[168:171], v[80:95]\n\t"
    "ds_read_b64_tr_b16 v[152:153], v233 offset:8192\n\t"
    "ds_read_b64_tr_b16 v[154:155], v233 offset:10240\n\t"
    "v_exp_f32 v49, v49\n\t"
    "v_add_f32 v246, v246, v48\n\t"
    "v_cvt_pk_bf16_f32 v9, v10, v11\n\t"
    "v_mfma_f32_32x32x16_bf16 v[96:111], a[232:235], a[136:139], v[96:111]\n\t"
    "ds_read_b64_tr_b16 v[168:169], v234 offset:8192\n\t"
    "ds_read_b64_tr_b16 v[170:171], v234 offset:10240\n\t"
    "v_exp_f32 v50, v50\n\t"
    "v_add_f32 v247, v247, v49\n\t"
    "v_cvt_pk_bf16_f32 v10, v12, v13\n\t"
    "v_mfma_f32_32x32x16_bf16 v[112:127], a[232:235], a[168:171], v[112:127]\n\t"
    "ds_read_b64_tr_b16 v[184:185], v235 offset:8192\n\t"
    "ds_read_b64_tr_b16 v[186:187], v235 offset:10240\n\t"
    "v_exp_f32 v51, v51\n\t"
    "v_add_f32 v246, v246, v50\n\t"
    "v_cvt_pk_bf16_f32 v11, v14, v15\n\t"
    "v_mfma_f32_32x32x16_bf16 v[64:79], a[204:207], a[140:143], v[64:79]\n\t"
    "ds_read_b64_tr_b16 v[140:141], v232 offset:12288\n\t"
    "ds_read_b64_tr_b16 v[142:143], v232 offset:14336\n\t"
    "v_exp_f32 v52, v52\n\t"
    "v_add_f32 v247, v247, v51\n\t"
    "v_cvt_pk_bf16_f32 v24, v24, v25\n\t"
    "v_mfma_f32_32x32x16_bf16 v[80:95], a[204:207], a[172:175], v[80:95]\n\t"
    "ds_read_b64_tr_b16 v[156:157], v233 offset:12288\n\t"
    "ds_read_b64_tr_b16 v[158:159], v233 offset:14336\n\t"
    "v_exp_f32 v53, v53\n\t"
    "v_add_f32 v246, v246, v52\n\t"
    "v_cvt_pk_bf16_f32 v25, v26, v27\n\t"
    "v_mfma_f32_32x32x16_bf16 v[96:111], a[236:239], a[140:143], v[96:111]\n\t"
    "ds_read_b64_tr_b16 v[172:173], v234 offset:12288\n\t"
    "ds_read_b64_tr_b16 v[174:175], v234 offset:14336\n\t"
    "v_exp_f32 v54, v54\n\t"
    "v_add_f32 v247, v247, v53\n\t"
    "v_cvt_pk_bf16_f32 v26, v28, v29\n\t"
    "v_mfma_f32_32x32x16_bf16 v[112:127], a[236:239], a[172:175], v[112:127]\n\t"
    "ds_read_b64_tr_b16 v[188:189], v235 offset:12288\n\t"
    "ds_read_b64_tr_b16 v[190:191], v235 offset:14336\n\t"
    "v_exp_f32 v55, v55\n\t"
    "v_add_f32 v246, v246, v54\n\t"
    "v_cvt_pk_bf16_f32 v27, v30, v31\n\t"
    "v_mfma_f32_32x32x16_bf16 v[64:79], a[208:211], a[144:147], v[64:79]\n\t"
    "v_exp_f32 v56, v56\n\t"
    "v_add_f32 v247, v247, v55\n\t"
    "v_cvt_pk_bf16_f32 v32, v32, v33\n\t"
    "v_mfma_f32_32x32x16_bf16 v[80:95], a[208:211], a[176:179], v[80:95]\n\t"
    "v_exp_f32 v57, v57\n\t"
    "v_add_f32 v246, v246, v56\n\t"
    "v_cvt_pk_bf16_f32 v33, v34, v35\n\t"
    "s_add_u32 m0, s40, 0x0\n\t"
    "v_mfma_f32_32x32x16_bf16 v[96:111], a[240:243], a[144:147], v[96:111]\n\t"
    "v_exp_f32 v58, v58\n\t"
    "v_add_f32 v247, v247, v57\n\t"
    "v_cvt_pk_bf16_f32 v34, v36, v37\n\t"
    "buffer_load_dwordx4 v236, %1, s41 offen offset:0 nt lds\n\t"
    "v_mfma_f32_32x32x16_bf16 v[112:127], a[240:243], a[176:179], v[112:127]\n\t"
    "v_exp_f32 v59, v59\n\t"
    "v_add_f32 v246, v246, v58\n\t"
    "v_cvt_pk_bf16_f32 v35, v38, v39\n\t"
    "buffer_load_dwordx4 v237, %1, s41 offen offset:1024 nt lds\n\t"
    "v_mfma_f32_32x32x16_bf16 v[64:79], a[212:215], a[148:151], v[64:79]\n\t"
    "v_exp_f32 v60, v60\n\t"
    "v_add_f32 v247, v247, v59\n\t"
    "v_cvt_pk_bf16_f32 v40, v40, v41\n\t"
    "v_mfma_f32_32x32x16_bf16 v[80:95], a[212:215], a[180:183], v[80:95]\n\t"
    "v_exp_f32 v61, v61\n\t"
    "v_add_f32 v246, v246, v60\n\t"
    "v_cvt_pk_bf16_f32 v41, v42, v43\n\t"
    "buffer_load_dwordx4 v238, %1, s41 offen offset:2048 nt lds\n\t"
    "v_mfma_f32_32x32x16_bf16 v[96:111], a[244:247], a[148:151], v[96:111]\n\t"
    "v_exp_f32 v62, v62\n\t"
    "v_add_f32 v247, v247, v61\n\t"
    "v_cvt_pk_bf16_f32 v42, v44, v45\n\t"
    "buffer_load_dwordx4 v239, %1, s41 offen offset:3072 nt lds\n\t"
    "v_mfma_f32_32x32x16_bf16 v[112:127], a[244:247], a[180:183], v[112:127]\n\t"
    "v_exp_f32 v63, v63\n\t"
    "v_add_f32 v246, v246, v62\n\t"
    "v_cvt_pk_bf16_f32 v43, v46, v47\n\t"
    "s_add_u32 s41, s41, 0x4000\n\t"
    "v_mfma_f32_32x32x16_bf16 v[64:79], a[216:219], a[152:155], v[64:79]\n\t"
    "v_add_f32 v247, v247, v63\n\t"
    "v_cvt_pk_bf16_f32 v48, v48, v49\n\t"
    "v_mfma_f32_32x32x16_bf16 v[80:95], a[216:219], a[184:187], v[80:95]\n\t"
    "v_cvt_pk_bf16_f32 v49, v50, v51\n\t"
    "s_add_u32 m0, s40, 0x1c000\n\t"
    "v_mfma_f32_32x32x16_bf16 v[96:111], a[248:251], a[152:155], v[96:111]\n\t"
    "v_cvt_pk_bf16_f32 v50, v52, v53\n\t"
    "buffer_load_dwordx4 v240, %2, s42 offen offset:0 nt lds\n\t"
    "v_mfma_f32_32x32x16_bf16 v[112:127], a[248:251], a[184:187], v[112:127]\n\t"
    "v_cvt_pk_bf16_f32 v51, v54, v55\n\t"
    "buffer_load_dwordx4 v240, %2, s42 offen offset:1024 nt lds\n\t"
    "v_mfma_f32_32x32x16_bf16 v[64:79], a[220:223], a[156:159], v[64:79]\n\t"
    "v_cvt_pk_bf16_f32 v56, v56, v57\n\t"
    "v_mfma_f32_32x32x16_bf16 v[80:95], a[220:223], a[188:191], v[80:95]\n\t"
    "v_cvt_pk_bf16_f32 v57, v58, v59\n\t"
    "buffer_load_dwordx4 v240, %2, s42 offen offset:2048 nt lds\n\t"
    "v_mfma_f32_32x32x16_bf16 v[96:111], a[252:255], a[156:159], v[96:111]\n\t"
    "v_cvt_pk_bf16_f32 v58, v60, v61\n\t"
    "buffer_load_dwordx4 v240, %2, s42 offen offset:3072 nt lds\n\t"
    "v_mfma_f32_32x32x16_bf16 v[112:127], a[252:255], a[188:191], v[112:127]\n\t"
    "v_cvt_pk_bf16_f32 v59, v62, v63\n\t"
    "s_add_u32 s42, s42, 0x4000\n\t"
    "s_waitcnt vmcnt(16) lgkmcnt(0)\n\t"
    "s_barrier\n\t"
    "v_mfma_f32_32x32x16_bf16 a[0:15], v[128:131], v[0:3], a[0:15]\n\t"
    "ds_read_b128 a[192:195], v224 offset:32768\n\t"
    "ds_read_b128 a[196:199], v225 offset:32768\n\t"
    "ds_read_b128 a[200:203], v226 offset:32768\n\t"
    "ds_read_b128 a[204:207], v227 offset:32768\n\t"
    "v_max3_f32 v248, v64, v65, v66\n\t"
    "v_max3_f32 v248, v248, v67, v68\n\t"
    "v_mfma_f32_32x32x16_bf16 a[16:31], v[128:131], v[16:19], a[16:31]\n\t"
    "ds_read_b128 a[208:211], v228 offset:32768\n\t"
    "ds_read_b128 a[212:215], v229 offset:32768\n\t"
    "ds_read_b128 a[216:219], v230 offset:32768\n\t"
    "ds_read_b128 a[220:223], v231 offset:32768\n\t"
    "v_max3_f32 v248, v248, v69, v70\n\t"
    "v_max3_f32 v248, v248, v71, v72\n\t"
    "v_mfma_f32_32x32x16_bf16 a[32:47], v[144:147], v[0:3], a[32:47]\n\t"
    "ds_read_b128 a[224:227], v224 offset:40960\n\t"
    "ds_read_b128 a[228:231], v225 offset:40960\n\t"
    "ds_read_b128 a[232:235], v226 offset:40960\n\t"
    "ds_read_b128 a[236:239], v227 offset:40960\n\t"
    "v_max3_f32 v248, v248, v73, v74\n\t"
    "v_max3_f32 v248, v248, v75, v76\n\t"
    "v_mfma_f32_32x32x16_bf16 a[48:63], v[144:147], v[16:19], a[48:63]\n\t"
    "ds_read_b128 a[240:243], v228 offset:40960\n\t"
    "ds_read_b128 a[244:247], v229 offset:40960\n\t"
    "ds_read_b128 a[248:251], v230 offset:40960\n\t"
    "ds_read_b128 a[252:255], v231 offset:40960\n\t"
    "v_max3_f32 v248, v248, v77, v78\n\t"
    "v_max3_f32 v248, v248, v79, v96\n\t"
    "v_mfma_f32_32x32x16_bf16 a[64:79], v[160:163], v[0:3], a[64:79]\n\t"
    "v_max3_f32 v248, v248, v97, v98\n\t"
    "v_max3_f32 v248, v248, v99, v100\n\t"
    "v_max3_f32 v248, v248, v101, v102\n\t"
    "v_max3_f32 v248, v248, v103, v104\n\t"
    "v_max3_f32 v248, v248, v105, v106\n\t"
    "v_max3_f32 v248, v248, v107, v108\n\t"
    "v_mfma_f32_32x32x16_bf16 a[80:95], v[160:163], v[16:19], a[80:95]\n\t"
    "v_max3_f32 v248, v248, v109, v110\n\t"
    "v_max_f32 v248, v248, v111\n\t"
    "v_max3_f32 v249, v80, v81, v82\n\t"
    "v_max3_f32 v249, v249, v83, v84\n\t"
    "v_max3_f32 v249, v249, v85, v86\n\t"
    "v_max3_f32 v249, v249, v87, v88\n\t"
    "v_mfma_f32_32x32x16_bf16 a[96:111], v[176:179], v[0:3], a[96:111]\n\t"
    "v_max3_f32 v249, v249, v89, v90\n\t"
    "v_max3_f32 v249, v249, v91, v92\n\t"
    "v_max3_f32 v249, v249, v93, v94\n\t"
    "v_max3_f32 v249, v249, v95, v112\n\t"
    "v_max3_f32 v249, v249, v113, v114\n\t"
    "v_max3_f32 v249, v249, v115, v116\n\t"
    "v_mfma_f32_32x32x16_bf16 a[112:127], v[176:179], v[16:19], a[112:127]\n\t"
    "v_max3_f32 v249, v249, v117, v118\n\t"
    "v_max3_f32 v249, v249, v119, v120\n\t"
    "v_max3_f32 v249, v249, v121, v122\n\t"
    "v_max3_f32 v249, v249, v123, v124\n\t"
    "v_max3_f32 v249, v249, v125, v126\n\t"
    "v_max_f32 v249, v249, v127\n\t"
    "v_mfma_f32_32x32x16_bf16 a[0:15], v[132:135], v[8:11], a[0:15]\n\t"
    "v_max_f32 v252, v248, v249\n\t"
    "v_cmp_lt_f32 vcc, s44, v252\n\t"
    "s_nop 1\n\t"
    "s_cbranch_vccnz RARE1_1%=\n\t"
    "RARE1R_1%=:\n\t"
    "v_mfma_f32_32x32x16_bf16 a[16:31], v[132:135], v[24:27], a[16:31]\n\t"
    "v_exp_f32 v64, v64\n\t"
    "v_exp_f32 v65, v65\n\t"
    "v_mfma_f32_32x32x16_bf16 a[32:47], v[148:151], v[8:11], a[32:47]\n\t"
    "v_exp_f32 v66, v66\n\t"
    "v_exp_f32 v67, v67\n\t"
    "v_add_f32 v244, v244, v64\n\t"
    "v_add_f32 v245, v245, v65\n\t"
    "v_mfma_f32_32x32x16_bf16 a[48:63], v[148:151], v[24:27], a[48:63]\n\t"
    "v_exp_f32 v68, v68\n\t"
    "v_exp_f32 v69, v69\n\t"
    "v_add_f32 v244, v244, v66\n\t"
    "v_add_f32 v245, v245, v67\n\t"
    "v_mfma_f32_32x32x16_bf16 a[64:79], v[164:167], v[8:11], a[64:79]\n\t"
    "v_exp_f32 v70, v70\n\t"
    "v_exp_f32 v71, v71\n\t"
    "v_add_f32 v244, v244, v68\n\t"
    "v_add_f32 v245, v245, v69\n\t"
    "v_mfma_f32_32x32x16_bf16 a[80:95], v[164:167], v[24:27], a[80:95]\n\t"
    "v_exp_f32 v72, v72\n\t"
    "v_exp_f32 v73, v73\n\t"
    "v_add_f32 v244, v244, v70\n\t"
    "v_add_f32 v245, v245, v71\n\t"
    "v_mfma_f32_32x32x16_bf16 a[96:111], v[180:183], v[8:11], a[96:111]\n\t"
    "v_exp_f32 v74, v74\n\t"
    "v_exp_f32 v75, v75\n\t"
    "v_add_f32 v244, v244, v72\n\t"
    "v_add_f32 v245, v245, v73\n\t"
    "v_mfma_f32_32x32x16_bf16 a[112:127], v[180:183], v[24:27], a[112:127]\n\t"
    "v_exp_f32 v76, v76\n\t"
    "v_exp_f32 v77, v77\n\t"
    "v_add_f32 v244, v244, v74\n\t"
    "v_add_f32 v245, v245, v75\n\t"
    "v_mfma_f32_32x32x16_bf16 a[0:15], v[136:139], v[32:35], a[0:15]\n\t"
    "v_exp_f32 v78, v78\n\t"
    "v_exp_f32 v79, v79\n\t"
    "v_add_f32 v244, v244, v76\n\t"
    "v_add_f32 v245, v245, v77\n\t"
    "v_mfma_f32_32x32x16_bf16 a[16:31], v[136:139], v[48:51], a[16:31]\n\t"
    "v_exp_f32 v80, v80\n\t"
    "v_exp_f32 v81, v81\n\t"
    "v_add_f32 v244, v244, v78\n\t"
    "v_add_f32 v245, v245, v79\n\t"
    "v_mfma_f32_32x32x16_bf16 a[32:47], v[152:155], v[32:35], a[32:47]\n\t"
    "v_exp_f32 v82, v82\n\t"
    "v_exp_f32 v83, v83\n\t"
    "v_add_f32 v246, v246, v80\n\t"
    "v_add_f32 v247, v247, v81\n\t"
    "v_mfma_f32_32x32x16_bf16 a[48:63], v[152:155], v[48:51], a[48:63]\n\t"
    "v_exp_f32 v84, v84\n\t"
    "v_exp_f32 v85, v85\n\t"
    "v_add_f32 v246, v246, v82\n\t"
    "v_add_f32 v247, v247, v83\n\t"
    "v_mfma_f32_32x32x16_bf16 a[64:79], v[168:171], v[32:35], a[64:79]\n\t"
    "v_exp_f32 v86, v86\n\t"
    "v_exp_f32 v87, v87\n\t"
    "v_add_f32 v246, v246, v84\n\t"
    "v_add_f32 v247, v247, v85\n\t"
    "v_mfma_f32_32x32x16_bf16 a[80:95], v[168:171], v[48:51], a[80:95]\n\t"
    "v_exp_f32 v88, v88\n\t"
    "v_exp_f32 v89, v89\n\t"
    "v_add_f32 v246, v246, v86\n\t"
    "v_add_f32 v247, v247, v87\n\t"
    "v_mfma_f32_32x32x16_bf16 a[96:111], v[184:187], v[32:35], a[96:111]\n\t"
    "v_exp_f32 v90, v90\n\t"
    "v_exp_f32 v91, v91\n\t"
    "v_add_f32 v246, v246, v88\n\t"
    "v_add_f32 v247, v247, v89\n\t"
    "v_mfma_f32_32x32x16_bf16 a[112:127], v[184:187], v[48:51], a[112:127]\n\t"
    "v_exp_f32 v92, v92\n\t"
    "v_exp_f32 v93, v93\n\t"
    "v_add_f32 v246, v246, v90\n\t"
    "v_add_f32 v247, v247, v91\n\t"
    "v_mfma_f32_32x32x16_bf16 a[0:15], v[140:143], v[40:43], a[0:15]\n\t"
    "v_exp_f32 v94, v94\n\t"
    "v_exp_f32 v95, v95\n\t"
    "v_add_f32 v246, v246, v92\n\t"
    "v_add_f32 v247, v247, v93\n\t"
    "v_mfma_f32_32x32x16_bf16 a[16:31], v[140:143], v[56:59], a[16:31]\n\t"
    "v_exp_f32 v96, v96\n\t"
    "v_exp_f32 v97, v97\n\t"
    "v_add_f32 v246, v246, v94\n\t"
    "v_add_f32 v247, v247, v95\n\t"
    "v_mfma_f32_32x32x16_bf16 a[32:47], v[156:159], v[40:43], a[32:47]\n\t"
    "v_exp_f32 v98, v98\n\t"
    "v_exp_f32 v99, v99\n\t"
    "v_add_f32 v244, v244, v96\n\t"
    "v_add_f32 v245, v245, v97\n\t"
    "v_mfma_f32_32x32x16_bf16 a[48:63], v[156:159], v[56:59], a[48:63]\n\t"
    "v_exp_f32 v100, v100\n\t"
    "v_exp_f32 v101, v101\n\t"
    "v_add_f32 v244, v244, v98\n\t"
    "v_add_f32 v245, v245, v99\n\t"
    "v_mfma_f32_32x32x16_bf16 a[64:79], v[172:175], v[40:43], a[64:79]\n\t"
    "v_exp_f32 v102, v102\n\t"
    "v_exp_f32 v103, v103\n\t"
    "v_add_f32 v244, v244, v100\n\t"
    "v_add_f32 v245, v245, v101\n\t"
    "v_mfma_f32_32x32x16_bf16 a[80:95], v[172:175], v[56:59], a[80:95]\n\t"
    "v_add_f32 v244, v244, v102\n\t"
    "v_add_f32 v245, v245, v103\n\t"
    "v_mfma_f32_32x32x16_bf16 a[96:111], v[188:191], v[40:43], a[96:111]\n\t"
    "v_mfma_f32_32x32x16_bf16 a[112:127], v[188:191], v[56:59], a[112:127]\n\t"
    "s_cmp_lg_u32 s46, 0\n\t"
    "s_cbranch_scc1 RARE2_1%=\n\t"
    "RARE2R_1%=:\n\t"
    "s_waitcnt lgkmcnt(0)\n\t"
    "v_mfma_f32_32x32x16_bf16 v[0:15], a[192:195], a[128:131], v[192:207]\n\t"
    "ds_read_b64_tr_b16 v[128:129], v232 offset:16384\n\t"
    "ds_read_b64_tr_b16 v[130:131], v232 offset:18432\n\t"
    "v_exp_f32 v104, v104\n\t"
    "v_cvt_pk_bf16_f32 v64, v64, v65\n\t"
    "v_mfma_f32_32x32x16_bf16 v[16:31], a[192:195], a[160:163], v[208:223]\n\t"
    "ds_read_b64_tr_b16 v[144:145], v233 offset:16384\n\t"
    "ds_read_b64_tr_b16 v[146:147], v233 offset:18432\n\t"
    "v_exp_f32 v105, v105\n\t"
    "v_add_f32 v244, v244, v104\n\t"
    "v_cvt_pk_bf16_f32 v65, v66, v67\n\t"
    "v_mfma_f32_32x32x16_bf16 v[32:47], a[224:227], a[128:131], v[192:207]\n\t"
    "ds_read_b64_tr_b16 v[160:161], v234 offset:16384\n\t"
    "ds_read_b64_tr_b16 v[162:163], v234 offset:18432\n\t"
    "v_exp_f32 v106, v106\n\t"
    "v_add_f32 v245, v245, v105\n\t"
    "v_cvt_pk_bf16_f32 v66, v68, v69\n\t"
    "v_mfma_f32_32x32x16_bf16 v[48:63], a[224:227], a[160:163], v[208:223]\n\t"
    "ds_read_b64_tr_b16 v[176:177], v235 offset:16384\n\t"
    "ds_read_b64_tr_b16 v[178:179], v235 offset:18432\n\t"
    "v_exp_f32 v107, v107\n\t"
    "v_add_f32 v244, v244, v106\n\t"
    "v_cvt_pk_bf16_f32 v67, v70, v71\n\t"
    "v_mfma_f32_32x32x16_bf16 v[0:15], a[196:199], a[132:135], v[0:15]\n\t"
    "ds_read_b64_tr_b16 v[132:133], v232 offset:20480\n\t"
    "ds_read_b64_tr_b16 v[134:135], v232 offset:22528\n\t"
    "v_exp_f32 v108, v108\n\t"
    "v_add_f32 v245, v245, v107\n\t"
    "v_cvt_pk_bf16_f32 v80, v80, v81\n\t"
    "v_mfma_f32_32x32x16_bf16 v[16:31], a[196:199], a[164:167], v[16:31]\n\t"
    "ds_read_b64_tr_b16 v[148:149], v233 offset:20480\n\t"
    "ds_read_b64_tr_b16 v[150:151], v233 offset:22528\n\t"
    "v_exp_f32 v109, v109\n\t"
    "v_add_f32 v244, v244, v108\n\t"
    "v_cvt_pk_bf16_f32 v81, v82, v83\n\t"
    "v_mfma_f32_32x32x16_bf16 v[32:47], a[228:231], a[132:135], v[32:47]\n\t"
    "ds_read_b64_tr_b16 v[164:165], v234 offset:20480\n\t"
    "ds_read_b64_tr_b16 v[166:167], v234 offset:22528\n\t"
    "v_exp_f32 v110, v110\n\t"
    "v_add_f32 v245, v245, v109\n\t"
    "v_cvt_pk_bf16_f32 v82, v84, v85\n\t"
    "v_mfma_f32_32x32x16_bf16 v[48:63], a[228:231], a[164:167], v[48:63]\n\t"
    "ds_read_b64_tr_b16 v[180:181], v235 offset:20480\n\t"
    "ds_read_b64_tr_b16 v[182:183], v235 offset:22528\n\t"
    "v_exp_f32 v111, v111\n\t"
    "v_add_f32 v244, v244, v110\n\t"
    "v_cvt_pk_bf16_f32 v83, v86, v87\n\t"
    "v_mfma_f32_32x32x16_bf16 v[0:15], a[200:203], a[136:139], v[0:15]\n\t"
    "ds_read_b64_tr_b16 v[136:137], v232 offset:24576\n\t"
    "ds_read_b64_tr_b16 v[138:139], v232 offset:26624\n\t"
    "v_exp_f32 v112, v112\n\t"
    "v_add_f32 v245, v245, v111\n\t"
    "v_cvt_pk_bf16_f32 v72, v72, v73\n\t"
    "v_mfma_f32_32x32x16_bf16 v[16:31], a[200:203], a[168:171], v[16:31]\n\t"
    "ds_read_b64_tr_b16 v[152:153], v233 offset:24576\n\t"
    "ds_read_b64_tr_b16 v[154:155], v233 offset:26624\n\t"
    "v_exp_f32 v113, v113\n\t"
    "v_add_f32 v246, v246, v112\n\t"
    "v_cvt_pk_bf16_f32 v73, v74, v75\n\t"
    "v_mfma_f32_32x32x16_bf16 v[32:47], a[232:235], a[136:139], v[32:47]\n\t"
    "ds_read_b64_tr_b16 v[168:169], v234 offset:24576\n\t"
    "ds_read_b64_tr_b16 v[170:171], v234 offset:26624\n\t"
    "v_exp_f32 v114, v114\n\t"
    "v_add_f32 v247, v247, v113\n\t"
    "v_cvt_pk_bf16_f32 v74, v76, v77\n\t"
    "v_mfma_f32_32x32x16_bf16 v[48:63], a[232:235], a[168:171], v[48:63]\n\t"
    "ds_read_b64_tr_b16 v[184:185], v235 offset:24576\n\t"
    "ds_read_b64_tr_b16 v[186:187], v235 offset:26624\n\t"
    "v_exp_f32 v115, v115\n\t"
    "v_add_f32 v246, v246, v114\n\t"
    "v_cvt_pk_bf16_f32 v75, v78, v79\n\t"
    "v_mfma_f32_32x32x16_bf16 v[0:15], a[204:207], a[140:143], v[0:15]\n\t"
    "ds_read_b64_tr_b16 v[140:141], v232 offset:28672\n\t"
    "ds_read_b64_tr_b16 v[142:143], v232 offset:30720\n\t"
    "v_exp_f32 v116, v116\n\t"
    "v_add_f32 v247, v247, v115\n\t"
    "v_cvt_pk_bf16_f32 v88, v88, v89\n\t"
    "v_mfma_f32_32x32x16_bf16 v[16:31], a[204:207], a[172:175], v[16:31]\n\t"
    "ds_read_b64_tr_b16 v[156:157], v233 offset:28672\n\t"
    "ds_read_b64_tr_b16 v[158:159], v233 offset:30720\n\t"
    "v_exp_f32 v117, v117\n\t"
    "v_add_f32 v246, v246, v116\n\t"
    "v_cvt_pk_bf16_f32 v89, v90, v91\n\t"
    "v_mfma_f32_32x32x16_bf16 v[32:47], a[236:239], a[140:143], v[32:47]\n\t"
    "ds_read_b64_tr_b16 v[172:173], v234 offset:28672\n\t"
    "ds_read_b64_tr_b16 v[174:175], v234 offset:30720\n\t"
    "v_exp_f32 v118, v118\n\t"
    "v_add_f32 v247, v247, v117\n\t"
    "v_cvt_pk_bf16_f32 v90, v92, v93\n\t"
    "v_mfma_f32_32x32x16_bf16 v[48:63], a[236:239], a[172:175], v[48:63]\n\t"
    "ds_read_b64_tr_b16 v[188:189], v235 offset:28672\n\t"
    "ds_read_b64_tr_b16 v[190:191], v235 offset:30720\n\t"
    "v_exp_f32 v119, v119\n\t"
    "v_add_f32 v246, v246, v118\n\t"
    "v_cvt_pk_bf16_f32 v91, v94, v95\n\t"
    "v_mfma_f32_32x32x16_bf16 v[0:15], a[208:211], a[144:147], v[0:15]\n\t"
    "v_exp_f32 v120, v120\n\t"
    "v_add_f32 v247, v247, v119\n\t"
    "v_cvt_pk_bf16_f32 v96, v96, v97\n\t"
    "v_mfma_f32_32x32x16_bf16 v[16:31], a[208:211], a[176:179], v[16:31]\n\t"
    "v_exp_f32 v121, v121\n\t"
    "v_add_f32 v246, v246, v120\n\t"
    "v_cvt_pk_bf16_f32 v97, v98, v99\n\t"
    "s_add_u32 m0, s40, 0x4000\n\t"
    "v_mfma_f32_32x32x16_bf16 v[32:47], a[240:243], a[144:147], v[32:47]\n\t"
    "v_exp_f32 v122, v122\n\t"
    "v_add_f32 v247, v247, v121\n\t"
    "v_cvt_pk_bf16_f32 v98, v100, v101\n\t"
    "buffer_load_dwordx4 v236, %1, s41 offen offset:0 nt lds\n\t"
    "v_mfma_f32_32x32x16_bf16 v[48:63], a[240:243], a[176:179], v[48:63]\n\t"
    "v_exp_f32 v123, v123\n\t"
    "v_add_f32 v246, v246, v122\n\t"
    "v_cvt_pk_bf16_f32 v99, v102, v103\n\t"
    "buffer_load_dwordx4 v237, %1, s41 offen offset:1024 nt lds\n\t"
    "v_mfma_f32_32x32x16_bf16 v[0:15], a[212:215], a[148:151], v[0:15]\n\t"
    "v_exp_f32 v124, v124\n\t"
    "v_add_f32 v247, v247, v123\n\t"
    "v_cvt_pk_bf16_f32 v104, v104, v105\n\t"
    "v_mfma_f32_32x32x16_bf16 v[16:31], a[212:215], a[180:183], v[16:31]\n\t"
    "v_exp_f32 v125, v125\n\t"
    "v_add_f32 v246, v246, v124\n\t"
    "v_cvt_pk_bf16_f32 v105, v106, v107\n\t"
    "buffer_load_dwordx4 v238, %1, s41 offen offset:2048 nt lds\n\t"
    "v_mfma_f32_32x32x16_bf16 v[32:47], a[244:247], a[148:151], v[32:47]\n\t"
    "v_exp_f32 v126, v126\n\t"
    "v_add_f32 v247, v247, v125\n\t"
    "v_cvt_pk_bf16_f32 v106, v108, v109\n\t"
    "buffer_load_dwordx4 v239, %1, s41 offen offset:3072 nt lds\n\t"
    "v_mfma_f32_32x32x16_bf16 v[48:63], a[244:247], a[180:183], v[48:63]\n\t"
    "v_exp_f32 v127, v127\n\t"
    "v_add_f32 v246, v246, v126\n\t"
    "v_cvt_pk_bf16_f32 v107, v110, v111\n\t"
    "s_add_u32 s41, s41, 0x4000\n\t"
    "v_mfma_f32_32x32x16_bf16 v[0:15], a[216:219], a[152:155], v[0:15]\n\t"
    "v_add_f32 v247, v247, v127\n\t"
    "v_cvt_pk_bf16_f32 v112, v112, v113\n\t"
    "v_mfma_f32_32x32x16_bf16 v[16:31], a[216:219], a[184:187], v[16:31]\n\t"
    "v_cvt_pk_bf16_f32 v113, v114, v115\n\t"
    "s_add_u32 m0, s40, 0x10000\n\t"
    "v_mfma_f32_32x32x16_bf16 v[32:47], a[248:251], a[152:155], v[32:47]\n\t"
    "v_cvt_pk_bf16_f32 v114, v116, v117\n\t"
    "buffer_load_dwordx4 v240, %2, s42 offen offset:0 nt lds\n\t"
    "v_mfma_f32_32x32x16_bf16 v[48:63], a[248:251], a[184:187], v[48:63]\n\t"
    "v_cvt_pk_bf16_f32 v115, v118, v119\n\t"
    "buffer_load_dwordx4 v240, %2, s42 offen offset:1024 nt lds\n\t"
    "v_mfma_f32_32x32x16_bf16 v[0:15], a[220:223], a[156:159], v[0:15]\n\t"
    "v_cvt_pk_bf16_f32 v120, v120, v121\n\t"
    "v_mfma_f32_32x32x16_bf16 v[16:31], a[220:223], a[188:191], v[16:31]\n\t"
    "v_cvt_pk_bf16_f32 v121, v122, v123\n\t"
    "buffer_load_dwordx4 v240, %2, s42 offen offset:2048 nt lds\n\t"
    "v_mfma_f32_32x32x16_bf16 v[32:47], a[252:255], a[156:159], v[32:47]\n\t"
    "v_cvt_pk_bf16_f32 v122, v124, v125\n\t"
    "buffer_load_dwordx4 v240, %2, s42 offen offset:3072 nt lds\n\t"
    "v_mfma_f32_32x32x16_bf16 v[48:63], a[252:255], a[188:191], v[48:63]\n\t"
    "v_cvt_pk_bf16_f32 v123, v126, v127\n\t"
    "s_add_u32 s42, s42, 0x4000\n\t"
    "s_waitcnt vmcnt(16) lgkmcnt(0)\n\t"
    "s_barrier\n\t"
    "v_mfma_f32_32x32x16_bf16 a[0:15], v[128:131], v[64:67], a[0:15]\n\t"
    "ds_read_b128 a[192:195], v224 offset:49152\n\t"
    "ds_read_b128 a[196:199], v225 offset:49152\n\t"
    "ds_read_b128 a[200:203], v226 offset:49152\n\t"
    "ds_read_b128 a[204:207], v227 offset:49152\n\t"
    "v_max3_f32 v248, v0, v1, v2\n\t"
    "v_max3_f32 v248, v248, v3, v4\n\t"
    "v_mfma_f32_32x32x16_bf16 a[16:31], v[128:131], v[80:83], a[16:31]\n\t"
    "ds_read_b128 a[208:211], v228 offset:49152\n\t"
    "ds_read_b128 a[212:215], v229 offset:49152\n\t"
    "ds_read_b128 a[216:219], v230 offset:49152\n\t"
    "ds_read_b128 a[220:223], v231 offset:49152\n\t"
    "v_max3_f32 v248, v248, v5, v6\n\t"
    "v_max3_f32 v248, v248, v7, v8\n\t"
    "v_mfma_f32_32x32x16_bf16 a[32:47], v[144:147], v[64:67], a[32:47]\n\t"
    "ds_read_b128 a[224:227], v224 offset:57344\n\t"
    "ds_read_b128 a[228:231], v225 offset:57344\n\t"
    "ds_read_b128 a[232:235], v226 offset:57344\n\t"
    "ds_read_b128 a[236:239], v227 offset:57344\n\t"
    "v_max3_f32 v248, v248, v9, v10\n\t"
    "v_max3_f32 v248, v248, v11, v12\n\t"
    "v_mfma_f32_32x32x16_bf16 a[48:63], v[144:147], v[80:83], a[48:63]\n\t"
    "ds_read_b128 a[240:243], v228 offset:57344\n\t"
    "ds_read_b128 a[244:247], v229 offset:57344\n\t"
    "ds_read_b128 a[248:251], v230 offset:57344\n\t"
    "ds_read_b128 a[252:255], v231 offset:57344\n\t"
    "v_max3_f32 v248, v248, v13, v14\n\t"
    "v_max3_f32 v248, v248, v15, v32\n\t"
    "v_mfma_f32_32x32x16_bf16 a[64:79], v[160:163], v[64:67], a[64:79]\n\t"
    "v_max3_f32 v248, v248, v33, v34\n\t"
    "v_max3_f32 v248, v248, v35, v36\n\t"
    "v_max3_f32 v248, v248, v37, v38\n\t"
    "v_max3_f32 v248, v248, v39, v40\n\t"
    "v_max3_f32 v248, v248, v41, v42\n\t"
    "v_max3_f32 v248, v248, v43, v44\n\t"
    "v_mfma_f32_32x32x16_bf16 a[80:95], v[160:163], v[80:83], a[80:95]\n\t"
    "v_max3_f32 v248, v248, v45, v46\n\t"
    "v_max_f32 v248, v248, v47\n\t"
    "v_max3_f32 v249, v16, v17, v18\n\t"
    "v_max3_f32 v249, v249, v19, v20\n\t"
    "v_max3_f32 v249, v249, v21, v22\n\t"
    "v_max3_f32 v249, v249, v23, v24\n\t"
    "v_mfma_f32_32x32x16_bf16 a[96:111], v[176:179], v[64:67], a[96:111]\n\t"
    "v_max3_f32 v249, v249, v25, v26\n\t"
    "v_max3_f32 v249, v249, v27, v28\n\t"
    "v_max3_f32 v249, v249, v29, v30\n\t"
    "v_max3_f32 v249, v249, v31, v48\n\t"
    "v_max3_f32 v249, v249, v49, v50\n\t"
    "v_max3_f32 v249, v249, v51, v52\n\t"
    "v_mfma_f32_32x32x16_bf16 a[112:127], v[176:179], v[80:83], a[112:127]\n\t"
    "v_max3_f32 v249, v249, v53, v54\n\t"
    "v_max3_f32 v249, v249, v55, v56\n\t"
    "v_max3_f32 v249, v249, v57, v58\n\t"
    "v_max3_f32 v249, v249, v59, v60\n\t"
    "v_max3_f32 v249, v249, v61, v62\n\t"
    "v_max_f32 v249, v249, v63\n\t"
    "v_mfma_f32_32x32x16_bf16 a[0:15], v[132:135], v[72:75], a[0:15]\n\t"
    "v_max_f32 v252, v248, v249\n\t"
    "v_cmp_lt_f32 vcc, s44, v252\n\t"
    "s_nop 1\n\t"
    "s_cbranch_vccnz RARE1_2%=\n\t"
    "RARE1R_2%=:\n\t"
    "v_mfma_f32_32x32x16_bf16 a[16:31], v[132:135], v[88:91], a[16:31]\n\t"
    "v_exp_f32 v0, v0\n\t"
    "v_exp_f32 v1, v1\n\t"
    "v_mfma_f32_32x32x16_bf16 a[32:47], v[148:151], v[72:75], a[32:47]\n\t"
    "v_exp_f32 v2, v2\n\t"
    "v_exp_f32 v3, v3\n\t"
    "v_add_f32 v244, v244, v0\n\t"
    "v_add_f32 v245, v245, v1\n\t"
    "v_mfma_f32_32x32x16_bf16 a[48:63], v[148:151], v[88:91], a[48:63]\n\t"
    "v_exp_f32 v4, v4\n\t"
    "v_exp_f32 v5, v5\n\t"
    "v_add_f32 v244, v244, v2\n\t"
    "v_add_f32 v245, v245, v3\n\t"
    "v_mfma_f32_32x32x16_bf16 a[64:79], v[164:167], v[72:75], a[64:79]\n\t"
    "v_exp_f32 v6, v6\n\t"
    "v_exp_f32 v7, v7\n\t"
    "v_add_f32 v244, v244, v4\n\t"
    "v_add_f32 v245, v245, v5\n\t"
    "v_mfma_f32_32x32x16_bf16 a[80:95], v[164:167], v[88:91], a[80:95]\n\t"
    "v_exp_f32 v8, v8\n\t"
    "v_exp_f32 v9, v9\n\t"
    "v_add_f32 v244, v244, v6\n\t"
    "v_add_f32 v245, v245, v7\n\t"
    "v_mfma_f32_32x32x16_bf16 a[96:111], v[180:183], v[72:75], a[96:111]\n\t"
    "v_exp_f32 v10, v10\n\t"
    "v_exp_f32 v11, v11\n\t"
    "v_add_f32 v244, v244, v8\n\t"
    "v_add_f32 v245, v245, v9\n\t"
    "v_mfma_f32_32x32x16_bf16 a[112:127], v[180:183], v[88:91], a[112:127]\n\t"
    "v_exp_f32 v12, v12\n\t"
    "v_exp_f32 v13, v13\n\t"
    "v_add_f32 v244, v244, v10\n\t"
    "v_add_f32 v245, v245, v11\n\t"
    "v_mfma_f32_32x32x16_bf16 a[0:15], v[136:139], v[96:99], a[0:15]\n\t"
    "v_exp_f32 v14, v14\n\t"
    "v_exp_f32 v15, v15\n\t"
    "v_add_f32 v244, v244, v12\n\t"
    "v_add_f32 v245, v245, v13\n\t"
    "v_mfma_f32_32x32x16_bf16 a[16:31], v[136:139], v[112:115], a[16:31]\n\t"
    "v_exp_f32 v16, v16\n\t"
    "v_exp_f32 v17, v17\n\t"
    "v_add_f32 v244, v244, v14\n\t"
    "v_add_f32 v245, v245, v15\n\t"
    "v_mfma_f32_32x32x16_bf16 a[32:47], v[152:155], v[96:99], a[32:47]\n\t"
    "v_exp_f32 v18, v18\n\t"
    "v_exp_f32 v19, v19\n\t"
    "v_add_f32 v246, v246, v16\n\t"
    "v_add_f32 v247, v247, v17\n\t"
    "v_mfma_f32_32x32x16_bf16 a[48:63], v[152:155], v[112:115], a[48:63]\n\t"
    "v_exp_f32 v20, v20\n\t"
    "v_exp_f32 v21, v21\n\t"
    "v_add_f32 v246, v246, v18\n\t"
    "v_add_f32 v247, v247, v19\n\t"
    "v_mfma_f32_32x32x16_bf16 a[64:79], v[168:171], v[96:99], a[64:79]\n\t"
    "v_exp_f32 v22, v22\n\t"
    "v_exp_f32 v23, v23\n\t"
    "v_add_f32 v246, v246, v20\n\t"
    "v_add_f32 v247, v247, v21\n\t"
    "v_mfma_f32_32x32x16_bf16 a[80:95], v[168:171], v[112:115], a[80:95]\n\t"
    "v_exp_f32 v24, v24\n\t"
    "v_exp_f32 v25, v25\n\t"
    "v_add_f32 v246, v246, v22\n\t"
    "v_add_f32 v247, v247, v23\n\t"
    "v_mfma_f32_32x32x16_bf16 a[96:111], v[184:187], v[96:99], a[96:111]\n\t"
    "v_exp_f32 v26, v26\n\t"
    "v_exp_f32 v27, v27\n\t"
    "v_add_f32 v246, v246, v24\n\t"
    "v_add_f32 v247, v247, v25\n\t"
    "v_mfma_f32_32x32x16_bf16 a[112:127], v[184:187], v[112:115], a[112:127]\n\t"
    "v_exp_f32 v28, v28\n\t"
    "v_exp_f32 v29, v29\n\t"
    "v_add_f32 v246, v246, v26\n\t"
    "v_add_f32 v247, v247, v27\n\t"
    "v_mfma_f32_32x32x16_bf16 a[0:15], v[140:143], v[104:107], a[0:15]\n\t"
    "v_exp_f32 v30, v30\n\t"
    "v_exp_f32 v31, v31\n\t"
    "v_add_f32 v246, v246, v28\n\t"
    "v_add_f32 v247, v247, v29\n\t"
    "v_mfma_f32_32x32x16_bf16 a[16:31], v[140:143], v[120:123], a[16:31]\n\t"
    "v_exp_f32 v32, v32\n\t"
    "v_exp_f32 v33, v33\n\t"
    "v_add_f32 v246, v246, v30\n\t"
    "v_add_f32 v247, v247, v31\n\t"
    "v_mfma_f32_32x32x16_bf16 a[32:47], v[156:159], v[104:107], a[32:47]\n\t"
    "v_exp_f32 v34, v34\n\t"
    "v_exp_f32 v35, v35\n\t"
    "v_add_f32 v244, v244, v32\n\t"
    "v_add_f32 v245, v245, v33\n\t"
    "v_mfma_f32_32x32x16_bf16 a[48:63], v[156:159], v[120:123], a[48:63]\n\t"
    "v_exp_f32 v36, v36\n\t"
    "v_exp_f32 v37, v37\n\t"
    "v_add_f32 v244, v244, v34\n\t"
    "v_add_f32 v245, v245, v35\n\t"
    "v_mfma_f32_32x32x16_bf16 a[64:79], v[172:175], v[104:107], a[64:79]\n\t"
    "v_exp_f32 v38, v38\n\t"
    "v_exp_f32 v39, v39\n\t"
    "v_add_f32 v244, v244, v36\n\t"
    "v_add_f32 v245, v245, v37\n\t"
    "v_mfma_f32_32x32x16_bf16 a[80:95], v[172:175], v[120:123], a[80:95]\n\t"
    "v_add_f32 v244, v244, v38\n\t"
    "v_add_f32 v245, v245, v39\n\t"
    "v_mfma_f32_32x32x16_bf16 a[96:111], v[188:191], v[104:107], a[96:111]\n\t"
    "v_mfma_f32_32x32x16_bf16 a[112:127], v[188:191], v[120:123], a[112:127]\n\t"
    "s_cmp_lg_u32 s46, 0\n\t"
    "s_cbranch_scc1 RARE2_2%=\n\t"
    "RARE2R_2%=:\n\t"
    "s_waitcnt lgkmcnt(0)\n\t"
    "v_mfma_f32_32x32x16_bf16 v[64:79], a[192:195], a[128:131], v[192:207]\n\t"
    "ds_read_b64_tr_b16 v[128:129], v232 offset:32768\n\t"
    "ds_read_b64_tr_b16 v[130:131], v232 offset:34816\n\t"
    "v_exp_f32 v40, v40\n\t"
    "v_cvt_pk_bf16_f32 v0, v0, v1\n\t"
    "v_mfma_f32_32x32x16_bf16 v[80:95], a[192:195], a[160:163], v[208:223]\n\t"
    "ds_read_b64_tr_b16 v[144:145], v233 offset:32768\n\t"
    "ds_read_b64_tr_b16 v[146:147], v233 offset:34816\n\t"
    "v_exp_f32 v41, v41\n\t"
    "v_add_f32 v244, v244, v40\n\t"
    "v_cvt_pk_bf16_f32 v1, v2, v3\n\t"
    "v_mfma_f32_32x32x16_bf16 v[96:111], a[224:227], a[128:131], v[192:207]\n\t"
    "ds_read_b64_tr_b16 v[160:161], v234 offset:32768\n\t"
    "ds_read_b64_tr_b16 v[162:163], v234 offset:34816\n\t"
    "v_exp_f32 v42, v42\n\t"
    "v_add_f32 v245, v245, v41\n\t"
    "v_cvt_pk_bf16_f32 v2, v4, v5\n\t"
    "v_mfma_f32_32x32x16_bf16 v[112:127], a[224:227], a[160:163], v[208:223]\n\t"
    "ds_read_b64_tr_b16 v[176:177], v235 offset:32768\n\t"
    "ds_read_b64_tr_b16 v[178:179], v235 offset:34816\n\t"
    "v_exp_f32 v43, v43\n\t"
    "v_add_f32 v244, v244, v42\n\t"
    "v_cvt_pk_bf16_f32 v3, v6, v7\n\t"
    "v_mfma_f32_32x32x16_bf16 v[64:79], a[196:199], a[132:135], v[64:79]\n\t"
    "ds_read_b64_tr_b16 v[132:133], v232 offset:36864\n\t"
    "ds_read_b64_tr_b16 v[134:135], v232 offset:38912\n\t"
    "v_exp_f32 v44, v44\n\t"
    "v_add_f32 v245, v245, v43\n\t"
    "v_cvt_pk_bf16_f32 v16, v16, v17\n\t"
    "v_mfma_f32_32x32x16_bf16 v[80:95], a[196:199], a[164:167], v[80:95]\n\t"
    "ds_read_b64_tr_b16 v[148:149], v233 offset:36864\n\t"
    "ds_read_b64_tr_b16 v[150:151], v233 offset:38912\n\t"
    "v_exp_f32 v45, v45\n\t"
    "v_add_f32 v244, v244, v44\n\t"
    "v_cvt_pk_bf16_f32 v17, v18, v19\n\t"
    "v_mfma_f32_32x32x16_bf16 v[96:111], a[228:231], a[132:135], v[96:111]\n\t"
    "ds_read_b64_tr_b16 v[164:165], v234 offset:36864\n\t"
    "ds_read_b64_tr_b16 v[166:167], v234 offset:38912\n\t"
    "v_exp_f32 v46, v46\n\t"
    "v_add_f32 v245, v245, v45\n\t"
    "v_cvt_pk_bf16_f32 v18, v20, v21\n\t"
    "v_mfma_f32_32x32x16_bf16 v[112:127], a[228:231], a[164:167], v[112:127]\n\t"
    "ds_read_b64_tr_b16 v[180:181], v235 offset:36864\n\t"
    "ds_read_b64_tr_b16 v[182:183], v235 offset:38912\n\t"
    "v_exp_f32 v47, v47\n\t"
    "v_add_f32 v244, v244, v46\n\t"
    "v_cvt_pk_bf16_f32 v19, v22, v23\n\t"
    "v_mfma_f32_32x32x16_bf16 v[64:79], a[200:203], a[136:139], v[64:79]\n\t"
    "ds_read_b64_tr_b16 v[136:137], v232 offset:40960\n\t"
    "ds_read_b64_tr_b16 v[138:139], v232 offset:43008\n\t"
    "v_exp_f32 v48, v48\n\t"
    "v_add_f32 v245, v245, v47\n\t"
    "v_cvt_pk_bf16_f32 v8, v8, v9\n\t"
    "v_mfma_f32_32x32x16_bf16 v[80:95], a[200:203], a[168:171], v[80:95]\n\t"
    "ds_read_b64_tr_b16 v[152:153], v233 offset:40960\n\t"
    "ds_read_b64_tr_b16 v[154:155], v233 offset:43008\n\t"
    "v_exp_f32 v49, v49\n\t"
    "v_add_f32 v246, v246, v48\n\t"
    "v_cvt_pk_bf16_f32 v9, v10, v11\n\t"
    "v_mfma_f32_32x32x16_bf16 v[96:111], a[232:235], a[136:139], v[96:111]\n\t"
    "ds_read_b64_tr_b16 v[168:169], v234 offset:40960\n\t"
    "ds_read_b64_tr_b16 v[170:171], v234 offset:43008\n\t"
    "v_exp_f32 v50, v50\n\t"
    "v_add_f32 v247, v247, v49\n\t"
    "v_cvt_pk_bf16_f32 v10, v12, v13\n\t"
    "v_mfma_f32_32x32x16_bf16 v[112:127], a[232:235], a[168:171], v[112:127]\n\t"
    "ds_read_b64_tr_b16 v[184:185], v235 offset:40960\n\t"
    "ds_read_b64_tr_b16 v[186:187], v235 offset:43008\n\t"
    "v_exp_f32 v51, v51\n\t"
    "v_add_f32 v246, v246, v50\n\t"
    "v_cvt_pk_bf16_f32 v11, v14, v15\n\t"
    "v_mfma_f32_32x32x16_bf16 v[64:79], a[204:207], a[140:143], v[64:79]\n\t"
    "ds_read_b64_tr_b16 v[140:141], v232 offset:45056\n\t"
    "ds_read_b64_tr_b16 v[142:143], v232 offset:47104\n\t"
    "v_exp_f32 v52, v52\n\t"
    "v_add_f32 v247, v247, v51\n\t"
    "v_cvt_pk_bf16_f32 v24, v24, v25\n\t"
    "v_mfma_f32_32x32x16_bf16 v[80:95], a[204:207], a[172:175], v[80:95]\n\t"
    "ds_read_b64_tr_b16 v[156:157], v233 offset:45056\n\t"
    "ds_read_b64_tr_b16 v[158:159], v233 offset:47104\n\t"
    "v_exp_f32 v53, v53\n\t"
    "v_add_f32 v246, v246, v52\n\t"
    "v_cvt_pk_bf16_f32 v25, v26, v27\n\t"
    "v_mfma_f32_32x32x16_bf16 v[96:111], a[236:239], a[140:143], v[96:111]\n\t"
    "ds_read_b64_tr_b16 v[172:173], v234 offset:45056\n\t"
    "ds_read_b64_tr_b16 v[174:175], v234 offset:47104\n\t"
    "v_exp_f32 v54, v54\n\t"
    "v_add_f32 v247, v247, v53\n\t"
    "v_cvt_pk_bf16_f32 v26, v28, v29\n\t"
    "v_mfma_f32_32x32x16_bf16 v[112:127], a[236:239], a[172:175], v[112:127]\n\t"
    "ds_read_b64_tr_b16 v[188:189], v235 offset:45056\n\t"
    "ds_read_b64_tr_b16 v[190:191], v235 offset:47104\n\t"
    "v_exp_f32 v55, v55\n\t"
    "v_add_f32 v246, v246, v54\n\t"
    "v_cvt_pk_bf16_f32 v27, v30, v31\n\t"
    "v_mfma_f32_32x32x16_bf16 v[64:79], a[208:211], a[144:147], v[64:79]\n\t"
    "v_exp_f32 v56, v56\n\t"
    "v_add_f32 v247, v247, v55\n\t"
    "v_cvt_pk_bf16_f32 v32, v32, v33\n\t"
    "v_mfma_f32_32x32x16_bf16 v[80:95], a[208:211], a[176:179], v[80:95]\n\t"
    "v_exp_f32 v57, v57\n\t"
    "v_add_f32 v246, v246, v56\n\t"
    "v_cvt_pk_bf16_f32 v33, v34, v35\n\t"
    "s_add_u32 m0, s40, 0x8000\n\t"
    "v_mfma_f32_32x32x16_bf16 v[96:111], a[240:243], a[144:147], v[96:111]\n\t"
    "v_exp_f32 v58, v58\n\t"
    "v_add_f32 v247, v247, v57\n\t"
    "v_cvt_pk_bf16_f32 v34, v36, v37\n\t"
    "buffer_load_dwordx4 v236, %1, s41 offen offset:0 nt lds\n\t"
    "v_mfma_f32_32x32x16_bf16 v[112:127], a[240:243], a[176:179], v[112:127]\n\t"
    "v_exp_f32 v59, v59\n\t"
    "v_add_f32 v246, v246, v58\n\t"
    "v_cvt_pk_bf16_f32 v35, v38, v39\n\t"
    "buffer_load_dwordx4 v237, %1, s41 offen offset:1024 nt lds\n\t"
    "v_mfma_f32_32x32x16_bf16 v[64:79], a[212:215], a[148:151], v[64:79]\n\t"
    "v_exp_f32 v60, v60\n\t"
    "v_add_f32 v247, v247, v59\n\t"
    "v_cvt_pk_bf16_f32 v40, v40, v41\n\t"
    "v_mfma_f32_32x32x16_bf16 v[80:95], a[212:215], a[180:183], v[80:95]\n\t"
    "v_exp_f32 v61, v61\n\t"
    "v_add_f32 v246, v246, v60\n\t"
    "v_cvt_pk_bf16_f32 v41, v42, v43\n\t"
    "buffer_load_dwordx4 v238, %1, s41 offen offset:2048 nt lds\n\t"
    "v_mfma_f32_32x32x16_bf16 v[96:111], a[244:247], a[148:151], v[96:111]\n\t"
    "v_exp_f32 v62, v62\n\t"
    "v_add_f32 v247, v247, v61\n\t"
    "v_cvt_pk_bf16_f32 v42, v44, v45\n\t"
    "buffer_load_dwordx4 v239, %1, s41 offen offset:3072 nt lds\n\t"
    "v_mfma_f32_32x32x16_bf16 v[112:127], a[244:247], a[180:183], v[112:127]\n\t"
    "v_exp_f32 v63, v63\n\t"
    "v_add_f32 v246, v246, v62\n\t"
    "v_cvt_pk_bf16_f32 v43, v46, v47\n\t"
    "s_add_u32 s41, s41, 0x4000\n\t"
    "v_mfma_f32_32x32x16_bf16 v[64:79], a[216:219], a[152:155], v[64:79]\n\t"
    "v_add_f32 v247, v247, v63\n\t"
    "v_cvt_pk_bf16_f32 v48, v48, v49\n\t"
    "v_mfma_f32_32x32x16_bf16 v[80:95], a[216:219], a[184:187], v[80:95]\n\t"
    "v_cvt_pk_bf16_f32 v49, v50, v51\n\t"
    "s_add_u32 m0, s40, 0x14000\n\t"
    "v_mfma_f32_32x32x16_bf16 v[96:111], a[248:251], a[152:155], v[96:111]\n\t"
    "v_cvt_pk_bf16_f32 v50, v52, v53\n\t"
    "buffer_load_dwordx4 v240, %2, s42 offen offset:0 nt lds\n\t"
    "v_mfma_f32_32x32x16_bf16 v[112:127], a[248:251], a[184:187], v[112:127]\n\t"
    "v_cvt_pk_bf16_f32 v51, v54, v55\n\t"
    "buffer_load_dwordx4 v240, %2, s42 offen offset:1024 nt lds\n\t"
    "v_mfma_f32_32x32x16_bf16 v[64:79], a[220:223], a[156:159], v[64:79]\n\t"
    "v_cvt_pk_bf16_f32 v56, v56, v57\n\t"
    "v_mfma_f32_32x32x16_bf16 v[80:95], a[220:223], a[188:191], v[80:95]\n\t"
    "v_cvt_pk_bf16_f32 v57, v58, v59\n\t"
    "buffer_load_dwordx4 v240, %2, s42 offen offset:2048 nt lds\n\t"
    "v_mfma_f32_32x32x16_bf16 v[96:111], a[252:255], a[156:159], v[96:111]\n\t"
    "v_cvt_pk_bf16_f32 v58, v60, v61\n\t"
    "buffer_load_dwordx4 v240, %2, s42 offen offset:3072 nt lds\n\t"
    "v_mfma_f32_32x32x16_bf16 v[112:127], a[252:255], a[188:191], v[112:127]\n\t"
    "v_cvt_pk_bf16_f32 v59, v62, v63\n\t"
    "s_add_u32 s42, s42, 0x4000\n\t"
    "s_waitcnt vmcnt(16) lgkmcnt(0)\n\t"
    "s_barrier\n\t"
    "v_mfma_f32_32x32x16_bf16 a[0:15], v[128:131], v[0:3], a[0:15]\n\t"
    "ds_read_b128 a[192:195], v224 offset:0\n\t"
    "ds_read_b128 a[196:199], v225 offset:0\n\t"
    "ds_read_b128 a[200:203], v226 offset:0\n\t"
    "ds_read_b128 a[204:207], v227 offset:0\n\t"
    "v_max3_f32 v248, v64, v65, v66\n\t"
    "v_max3_f32 v248, v248, v67, v68\n\t"
    "v_mfma_f32_32x32x16_bf16 a[16:31], v[128:131], v[16:19], a[16:31]\n\t"
    "ds_read_b128 a[208:211], v228 offset:0\n\t"
    "ds_read_b128 a[212:215], v229 offset:0\n\t"
    "ds_read_b128 a[216:219], v230 offset:0\n\t"
    "ds_read_b128 a[220:223], v231 offset:0\n\t"
    "v_max3_f32 v248, v248, v69, v70\n\t"
    "v_max3_f32 v248, v248, v71, v72\n\t"
    "v_mfma_f32_32x32x16_bf16 a[32:47], v[144:147], v[0:3], a[32:47]\n\t"
    "ds_read_b128 a[224:227], v224 offset:8192\n\t"
    "ds_read_b128 a[228:231], v225 offset:8192\n\t"
    "ds_read_b128 a[232:235], v226 offset:8192\n\t"
    "ds_read_b128 a[236:239], v227 offset:8192\n\t"
    "v_max3_f32 v248, v248, v73, v74\n\t"
    "v_max3_f32 v248, v248, v75, v76\n\t"
    "v_mfma_f32_32x32x16_bf16 a[48:63], v[144:147], v[16:19], a[48:63]\n\t"
    "ds_read_b128 a[240:243], v228 offset:8192\n\t"
    "ds_read_b128 a[244:247], v229 offset:8192\n\t"
    "ds_read_b128 a[248:251], v230 offset:8192\n\t"
    "ds_read_b128 a[252:255], v231 offset:8192\n\t"
    "v_max3_f32 v248, v248, v77, v78\n\t"
    "v_max3_f32 v248, v248, v79, v96\n\t"
    "v_mfma_f32_32x32x16_bf16 a[64:79], v[160:163], v[0:3], a[64:79]\n\t"
    "v_max3_f32 v248, v248, v97, v98\n\t"
    "v_max3_f32 v248, v248, v99, v100\n\t"
    "v_max3_f32 v248, v248, v101, v102\n\t"
    "v_max3_f32 v248, v248, v103, v104\n\t"
    "v_max3_f32 v248, v248, v105, v106\n\t"
    "v_max3_f32 v248, v248, v107, v108\n\t"
    "v_mfma_f32_32x32x16_bf16 a[80:95], v[160:163], v[16:19], a[80:95]\n\t"
    "v_max3_f32 v248, v248, v109, v110\n\t"
    "v_max_f32 v248, v248, v111\n\t"
    "v_max3_f32 v249, v80, v81, v82\n\t"
    "v_max3_f32 v249, v249, v83, v84\n\t"
    "v_max3_f32 v249, v249, v85, v86\n\t"
    "v_max3_f32 v249, v249, v87, v88\n\t"
    "v_mfma_f32_32x32x16_bf16 a[96:111], v[176:179], v[0:3], a[96:111]\n\t"
    "v_max3_f32 v249, v249, v89, v90\n\t"
    "v_max3_f32 v249, v249, v91, v92\n\t"
    "v_max3_f32 v249, v249, v93, v94\n\t"
    "v_max3_f32 v249, v249, v95, v112\n\t"
    "v_max3_f32 v249, v249, v113, v114\n\t"
    "v_max3_f32 v249, v249, v115, v116\n\t"
    "v_mfma_f32_32x32x16_bf16 a[112:127], v[176:179], v[16:19], a[112:127]\n\t"
    "v_max3_f32 v249, v249, v117, v118\n\t"
    "v_max3_f32 v249, v249, v119, v120\n\t"
    "v_max3_f32 v249, v249, v121, v122\n\t"
    "v_max3_f32 v249, v249, v123, v124\n\t"
    "v_max3_f32 v249, v249, v125, v126\n\t"
    "v_max_f32 v249, v249, v127\n\t"
    "v_mfma_f32_32x32x16_bf16 a[0:15], v[132:135], v[8:11], a[0:15]\n\t"
    "v_max_f32 v252, v248, v249\n\t"
    "v_cmp_lt_f32 vcc, s44, v252\n\t"
    "s_nop 1\n\t"
    "s_cbranch_vccnz RARE1_3%=\n\t"
    "RARE1R_3%=:\n\t"
    "v_mfma_f32_32x32x16_bf16 a[16:31], v[132:135], v[24:27], a[16:31]\n\t"
    "v_exp_f32 v64, v64\n\t"
    "v_exp_f32 v65, v65\n\t"
    "v_mfma_f32_32x32x16_bf16 a[32:47], v[148:151], v[8:11], a[32:47]\n\t"
    "v_exp_f32 v66, v66\n\t"
    "v_exp_f32 v67, v67\n\t"
    "v_add_f32 v244, v244, v64\n\t"
    "v_add_f32 v245, v245, v65\n\t"
    "v_mfma_f32_32x32x16_bf16 a[48:63], v[148:151], v[24:27], a[48:63]\n\t"
    "v_exp_f32 v68, v68\n\t"
    "v_exp_f32 v69, v69\n\t"
    "v_add_f32 v244, v244, v66\n\t"
    "v_add_f32 v245, v245, v67\n\t"
    "v_mfma_f32_32x32x16_bf16 a[64:79], v[164:167], v[8:11], a[64:79]\n\t"
    "v_exp_f32 v70, v70\n\t"
    "v_exp_f32 v71, v71\n\t"
    "v_add_f32 v244, v244, v68\n\t"
    "v_add_f32 v245, v245, v69\n\t"
    "v_mfma_f32_32x32x16_bf16 a[80:95], v[164:167], v[24:27], a[80:95]\n\t"
    "v_exp_f32 v72, v72\n\t"
    "v_exp_f32 v73, v73\n\t"
    "v_add_f32 v244, v244, v70\n\t"
    "v_add_f32 v245, v245, v71\n\t"
    "v_mfma_f32_32x32x16_bf16 a[96:111], v[180:183], v[8:11], a[96:111]\n\t"
    "v_exp_f32 v74, v74\n\t"
    "v_exp_f32 v75, v75\n\t"
    "v_add_f32 v244, v244, v72\n\t"
    "v_add_f32 v245, v245, v73\n\t"
    "v_mfma_f32_32x32x16_bf16 a[112:127], v[180:183], v[24:27], a[112:127]\n\t"
    "v_exp_f32 v76, v76\n\t"
    "v_exp_f32 v77, v77\n\t"
    "v_add_f32 v244, v244, v74\n\t"
    "v_add_f32 v245, v245, v75\n\t"
    "v_mfma_f32_32x32x16_bf16 a[0:15], v[136:139], v[32:35], a[0:15]\n\t"
    "v_exp_f32 v78, v78\n\t"
    "v_exp_f32 v79, v79\n\t"
    "v_add_f32 v244, v244, v76\n\t"
    "v_add_f32 v245, v245, v77\n\t"
    "v_mfma_f32_32x32x16_bf16 a[16:31], v[136:139], v[48:51], a[16:31]\n\t"
    "v_exp_f32 v80, v80\n\t"
    "v_exp_f32 v81, v81\n\t"
    "v_add_f32 v244, v244, v78\n\t"
    "v_add_f32 v245, v245, v79\n\t"
    "v_mfma_f32_32x32x16_bf16 a[32:47], v[152:155], v[32:35], a[32:47]\n\t"
    "v_exp_f32 v82, v82\n\t"
    "v_exp_f32 v83, v83\n\t"
    "v_add_f32 v246, v246, v80\n\t"
    "v_add_f32 v247, v247, v81\n\t"
    "v_mfma_f32_32x32x16_bf16 a[48:63], v[152:155], v[48:51], a[48:63]\n\t"
    "v_exp_f32 v84, v84\n\t"
    "v_exp_f32 v85, v85\n\t"
    "v_add_f32 v246, v246, v82\n\t"
    "v_add_f32 v247, v247, v83\n\t"
    "v_mfma_f32_32x32x16_bf16 a[64:79], v[168:171], v[32:35], a[64:79]\n\t"
    "v_exp_f32 v86, v86\n\t"
    "v_exp_f32 v87, v87\n\t"
    "v_add_f32 v246, v246, v84\n\t"
    "v_add_f32 v247, v247, v85\n\t"
    "v_mfma_f32_32x32x16_bf16 a[80:95], v[168:171], v[48:51], a[80:95]\n\t"
    "v_exp_f32 v88, v88\n\t"
    "v_exp_f32 v89, v89\n\t"
    "v_add_f32 v246, v246, v86\n\t"
    "v_add_f32 v247, v247, v87\n\t"
    "v_mfma_f32_32x32x16_bf16 a[96:111], v[184:187], v[32:35], a[96:111]\n\t"
    "v_exp_f32 v90, v90\n\t"
    "v_exp_f32 v91, v91\n\t"
    "v_add_f32 v246, v246, v88\n\t"
    "v_add_f32 v247, v247, v89\n\t"
    "v_mfma_f32_32x32x16_bf16 a[112:127], v[184:187], v[48:51], a[112:127]\n\t"
    "v_exp_f32 v92, v92\n\t"
    "v_exp_f32 v93, v93\n\t"
    "v_add_f32 v246, v246, v90\n\t"
    "v_add_f32 v247, v247, v91\n\t"
    "v_mfma_f32_32x32x16_bf16 a[0:15], v[140:143], v[40:43], a[0:15]\n\t"
    "v_exp_f32 v94, v94\n\t"
    "v_exp_f32 v95, v95\n\t"
    "v_add_f32 v246, v246, v92\n\t"
    "v_add_f32 v247, v247, v93\n\t"
    "v_mfma_f32_32x32x16_bf16 a[16:31], v[140:143], v[56:59], a[16:31]\n\t"
    "v_exp_f32 v96, v96\n\t"
    "v_exp_f32 v97, v97\n\t"
    "v_add_f32 v246, v246, v94\n\t"
    "v_add_f32 v247, v247, v95\n\t"
    "v_mfma_f32_32x32x16_bf16 a[32:47], v[156:159], v[40:43], a[32:47]\n\t"
    "v_exp_f32 v98, v98\n\t"
    "v_exp_f32 v99, v99\n\t"
    "v_add_f32 v244, v244, v96\n\t"
    "v_add_f32 v245, v245, v97\n\t"
    "v_mfma_f32_32x32x16_bf16 a[48:63], v[156:159], v[56:59], a[48:63]\n\t"
    "v_exp_f32 v100, v100\n\t"
    "v_exp_f32 v101, v101\n\t"
    "v_add_f32 v244, v244, v98\n\t"
    "v_add_f32 v245, v245, v99\n\t"
    "v_mfma_f32_32x32x16_bf16 a[64:79], v[172:175], v[40:43], a[64:79]\n\t"
    "v_exp_f32 v102, v102\n\t"
    "v_exp_f32 v103, v103\n\t"
    "v_add_f32 v244, v244, v100\n\t"
    "v_add_f32 v245, v245, v101\n\t"
    "v_mfma_f32_32x32x16_bf16 a[80:95], v[172:175], v[56:59], a[80:95]\n\t"
    "v_add_f32 v244, v244, v102\n\t"
    "v_add_f32 v245, v245, v103\n\t"
    "v_mfma_f32_32x32x16_bf16 a[96:111], v[188:191], v[40:43], a[96:111]\n\t"
    "v_mfma_f32_32x32x16_bf16 a[112:127], v[188:191], v[56:59], a[112:127]\n\t"
    "s_cmp_lg_u32 s46, 0\n\t"
    "s_cbranch_scc1 RARE2_3%=\n\t"
    "RARE2R_3%=:\n\t"
    "s_waitcnt lgkmcnt(0)\n\t"
    "v_mfma_f32_32x32x16_bf16 v[0:15], a[192:195], a[128:131], v[192:207]\n\t"
    "ds_read_b64_tr_b16 v[128:129], v232 offset:49152\n\t"
    "ds_read_b64_tr_b16 v[130:131], v232 offset:51200\n\t"
    "v_exp_f32 v104, v104\n\t"
    "v_cvt_pk_bf16_f32 v64, v64, v65\n\t"
    "v_mfma_f32_32x32x16_bf16 v[16:31], a[192:195], a[160:163], v[208:223]\n\t"
    "ds_read_b64_tr_b16 v[144:145], v233 offset:49152\n\t"
    "ds_read_b64_tr_b16 v[146:147], v233 offset:51200\n\t"
    "v_exp_f32 v105, v105\n\t"
    "v_add_f32 v244, v244, v104\n\t"
    "v_cvt_pk_bf16_f32 v65, v66, v67\n\t"
    "v_mfma_f32_32x32x16_bf16 v[32:47], a[224:227], a[128:131], v[192:207]\n\t"
    "ds_read_b64_tr_b16 v[160:161], v234 offset:49152\n\t"
    "ds_read_b64_tr_b16 v[162:163], v234 offset:51200\n\t"
    "v_exp_f32 v106, v106\n\t"
    "v_add_f32 v245, v245, v105\n\t"
    "v_cvt_pk_bf16_f32 v66, v68, v69\n\t"
    "v_mfma_f32_32x32x16_bf16 v[48:63], a[224:227], a[160:163], v[208:223]\n\t"
    "ds_read_b64_tr_b16 v[176:177], v235 offset:49152\n\t"
    "ds_read_b64_tr_b16 v[178:179], v235 offset:51200\n\t"
    "v_exp_f32 v107, v107\n\t"
    "v_add_f32 v244, v244, v106\n\t"
    "v_cvt_pk_bf16_f32 v67, v70, v71\n\t"
    "v_mfma_f32_32x32x16_bf16 v[0:15], a[196:199], a[132:135], v[0:15]\n\t"
    "ds_read_b64_tr_b16 v[132:133], v232 offset:53248\n\t"
    "ds_read_b64_tr_b16 v[134:135], v232 offset:55296\n\t"
    "v_exp_f32 v108, v108\n\t"
    "v_add_f32 v245, v245, v107\n\t"
    "v_cvt_pk_bf16_f32 v80, v80, v81\n\t"
    "v_mfma_f32_32x32x16_bf16 v[16:31], a[196:199], a[164:167], v[16:31]\n\t"
    "ds_read_b64_tr_b16 v[148:149], v233 offset:53248\n\t"
    "ds_read_b64_tr_b16 v[150:151], v233 offset:55296\n\t"
    "v_exp_f32 v109, v109\n\t"
    "v_add_f32 v244, v244, v108\n\t"
    "v_cvt_pk_bf16_f32 v81, v82, v83\n\t"
    "v_mfma_f32_32x32x16_bf16 v[32:47], a[228:231], a[132:135], v[32:47]\n\t"
    "ds_read_b64_tr_b16 v[164:165], v234 offset:53248\n\t"
    "ds_read_b64_tr_b16 v[166:167], v234 offset:55296\n\t"
    "v_exp_f32 v110, v110\n\t"
    "v_add_f32 v245, v245, v109\n\t"
    "v_cvt_pk_bf16_f32 v82, v84, v85\n\t"
    "v_mfma_f32_32x32x16_bf16 v[48:63], a[228:231], a[164:167], v[48:63]\n\t"
    "ds_read_b64_tr_b16 v[180:181], v235 offset:53248\n\t"
    "ds_read_b64_tr_b16 v[182:183], v235 offset:55296\n\t"
    "v_exp_f32 v111, v111\n\t"
    "v_add_f32 v244, v244, v110\n\t"
    "v_cvt_pk_bf16_f32 v83, v86, v87\n\t"
    "v_mfma_f32_32x32x16_bf16 v[0:15], a[200:203], a[136:139], v[0:15]\n\t"
    "ds_read_b64_tr_b16 v[136:137], v232 offset:57344\n\t"
    "ds_read_b64_tr_b16 v[138:139], v232 offset:59392\n\t"
    "v_exp_f32 v112, v112\n\t"
    "v_add_f32 v245, v245, v111\n\t"
    "v_cvt_pk_bf16_f32 v72, v72, v73\n\t"
    "v_mfma_f32_32x32x16_bf16 v[16:31], a[200:203], a[168:171], v[16:31]\n\t"
    "ds_read_b64_tr_b16 v[152:153], v233 offset:57344\n\t"
    "ds_read_b64_tr_b16 v[154:155], v233 offset:59392\n\t"
    "v_exp_f32 v113, v113\n\t"
    "v_add_f32 v246, v246, v112\n\t"
    "v_cvt_pk_bf16_f32 v73, v74, v75\n\t"
    "v_mfma_f32_32x32x16_bf16 v[32:47], a[232:235], a[136:139], v[32:47]\n\t"
    "ds_read_b64_tr_b16 v[168:169], v234 offset:57344\n\t"
    "ds_read_b64_tr_b16 v[170:171], v234 offset:59392\n\t"
    "v_exp_f32 v114, v114\n\t"
    "v_add_f32 v247, v247, v113\n\t"
    "v_cvt_pk_bf16_f32 v74, v76, v77\n\t"
    "v_mfma_f32_32x32x16_bf16 v[48:63], a[232:235], a[168:171], v[48:63]\n\t"
    "ds_read_b64_tr_b16 v[184:185], v235 offset:57344\n\t"
    "ds_read_b64_tr_b16 v[186:187], v235 offset:59392\n\t"
    "v_exp_f32 v115, v115\n\t"
    "v_add_f32 v246, v246, v114\n\t"
    "v_cvt_pk_bf16_f32 v75, v78, v79\n\t"
    "v_mfma_f32_32x32x16_bf16 v[0:15], a[204:207], a[140:143], v[0:15]\n\t"
    "ds_read_b64_tr_b16 v[140:141], v232 offset:61440\n\t"
    "ds_read_b64_tr_b16 v[142:143], v232 offset:63488\n\t"
    "v_exp_f32 v116, v116\n\t"
    "v_add_f32 v247, v247, v115\n\t"
    "v_cvt_pk_bf16_f32 v88, v88, v89\n\t"
    "v_mfma_f32_32x32x16_bf16 v[16:31], a[204:207], a[172:175], v[16:31]\n\t"
    "ds_read_b64_tr_b16 v[156:157], v233 offset:61440\n\t"
    "ds_read_b64_tr_b16 v[158:159], v233 offset:63488\n\t"
    "v_exp_f32 v117, v117\n\t"
    "v_add_f32 v246, v246, v116\n\t"
    "v_cvt_pk_bf16_f32 v89, v90, v91\n\t"
    "v_mfma_f32_32x32x16_bf16 v[32:47], a[236:239], a[140:143], v[32:47]\n\t"
    "ds_read_b64_tr_b16 v[172:173], v234 offset:61440\n\t"
    "ds_read_b64_tr_b16 v[174:175], v234 offset:63488\n\t"
    "v_exp_f32 v118, v118\n\t"
    "v_add_f32 v247, v247, v117\n\t"
    "v_cvt_pk_bf16_f32 v90, v92, v93\n\t"
    "v_mfma_f32_32x32x16_bf16 v[48:63], a[236:239], a[172:175], v[48:63]\n\t"
    "ds_read_b64_tr_b16 v[188:189], v235 offset:61440\n\t"
    "ds_read_b64_tr_b16 v[190:191], v235 offset:63488\n\t"
    "v_exp_f32 v119, v119\n\t"
    "v_add_f32 v246, v246, v118\n\t"
    "v_cvt_pk_bf16_f32 v91, v94, v95\n\t"
    "v_mfma_f32_32x32x16_bf16 v[0:15], a[208:211], a[144:147], v[0:15]\n\t"
    "v_exp_f32 v120, v120\n\t"
    "v_add_f32 v247, v247, v119\n\t"
    "v_cvt_pk_bf16_f32 v96, v96, v97\n\t"
    "v_mfma_f32_32x32x16_bf16 v[16:31], a[208:211], a[176:179], v[16:31]\n\t"
    "v_exp_f32 v121, v121\n\t"
    "v_add_f32 v246, v246, v120\n\t"
    "v_cvt_pk_bf16_f32 v97, v98, v99\n\t"
    "s_add_u32 m0, s40, 0xc000\n\t"
    "v_mfma_f32_32x32x16_bf16 v[32:47], a[240:243], a[144:147], v[32:47]\n\t"
    "v_exp_f32 v122, v122\n\t"
    "v_add_f32 v247, v247, v121\n\t"
    "v_cvt_pk_bf16_f32 v98, v100, v101\n\t"
    "buffer_load_dwordx4 v236, %1, s41 offen offset:0 nt lds\n\t"
    "v_mfma_f32_32x32x16_bf16 v[48:63], a[240:243], a[176:179], v[48:63]\n\t"
    "v_exp_f32 v123, v123\n\t"
    "v_add_f32 v246, v246, v122\n\t"
    "v_cvt_pk_bf16_f32 v99, v102, v103\n\t"
    "buffer_load_dwordx4 v237, %1, s41 offen offset:1024 nt lds\n\t"
    "v_mfma_f32_32x32x16_bf16 v[0:15], a[212:215], a[148:151], v[0:15]\n\t"
    "v_exp_f32 v124, v124\n\t"
    "v_add_f32 v247, v247, v123\n\t"
    "v_cvt_pk_bf16_f32 v104, v104, v105\n\t"
    "v_mfma_f32_32x32x16_bf16 v[16:31], a[212:215], a[180:183], v[16:31]\n\t"
    "v_exp_f32 v125, v125\n\t"
    "v_add_f32 v246, v246, v124\n\t"
    "v_cvt_pk_bf16_f32 v105, v106, v107\n\t"
    "buffer_load_dwordx4 v238, %1, s41 offen offset:2048 nt lds\n\t"
    "v_mfma_f32_32x32x16_bf16 v[32:47], a[244:247], a[148:151], v[32:47]\n\t"
    "v_exp_f32 v126, v126\n\t"
    "v_add_f32 v247, v247, v125\n\t"
    "v_cvt_pk_bf16_f32 v106, v108, v109\n\t"
    "buffer_load_dwordx4 v239, %1, s41 offen offset:3072 nt lds\n\t"
    "v_mfma_f32_32x32x16_bf16 v[48:63], a[244:247], a[180:183], v[48:63]\n\t"
    "v_exp_f32 v127, v127\n\t"
    "v_add_f32 v246, v246, v126\n\t"
    "v_cvt_pk_bf16_f32 v107, v110, v111\n\t"
    "s_add_u32 s41, s41, 0x4000\n\t"
    "v_mfma_f32_32x32x16_bf16 v[0:15], a[216:219], a[152:155], v[0:15]\n\t"
    "v_add_f32 v247, v247, v127\n\t"
    "v_cvt_pk_bf16_f32 v112, v112, v113\n\t"
    "v_mfma_f32_32x32x16_bf16 v[16:31], a[216:219], a[184:187], v[16:31]\n\t"
    "v_cvt_pk_bf16_f32 v113, v114, v115\n\t"
    "s_add_u32 m0, s40, 0x18000\n\t"
    "v_mfma_f32_32x32x16_bf16 v[32:47], a[248:251], a[152:155], v[32:47]\n\t"
    "v_cvt_pk_bf16_f32 v114, v116, v117\n\t"
    "buffer_load_dwordx4 v240, %2, s42 offen offset:0 nt lds\n\t"
    "v_mfma_f32_32x32x16_bf16 v[48:63], a[248:251], a[184:187], v[48:63]\n\t"
    "v_cvt_pk_bf16_f32 v115, v118, v119\n\t"
    "buffer_load_dwordx4 v240, %2, s42 offen offset:1024 nt lds\n\t"
    "v_mfma_f32_32x32x16_bf16 v[0:15], a[220:223], a[156:159], v[0:15]\n\t"
    "v_cvt_pk_bf16_f32 v120, v120, v121\n\t"
    "v_mfma_f32_32x32x16_bf16 v[16:31], a[220:223], a[188:191], v[16:31]\n\t"
    "v_cvt_pk_bf16_f32 v121, v122, v123\n\t"
    "buffer_load_dwordx4 v240, %2, s42 offen offset:2048 nt lds\n\t"
    "v_mfma_f32_32x32x16_bf16 v[32:47], a[252:255], a[156:159], v[32:47]\n\t"
    "v_cvt_pk_bf16_f32 v122, v124, v125\n\t"
    "buffer_load_dwordx4 v240, %2, s42 offen offset:3072 nt lds\n\t"
    "v_mfma_f32_32x32x16_bf16 v[48:63], a[252:255], a[188:191], v[48:63]\n\t"
    "v_cvt_pk_bf16_f32 v123, v126, v127\n\t"
    "s_add_u32 s42, s42, 0x4000\n\t"
    "s_add_u32 s43, s43, 1\n\t"
    "s_cmp_eq_u32 s43, 4\n\t"
    "s_cbranch_scc1 TAIL%=\n\t"
    "s_waitcnt vmcnt(16) lgkmcnt(0)\n\t"
    "s_barrier\n\t"
    "v_mfma_f32_32x32x16_bf16 a[0:15], v[128:131], v[64:67], a[0:15]\n\t"
    "ds_read_b128 a[192:195], v224 offset:16384\n\t"
    "ds_read_b128 a[196:199], v225 offset:16384\n\t"
    "ds_read_b128 a[200:203], v226 offset:16384\n\t"
    "ds_read_b128 a[204:207], v227 offset:16384\n\t"
    "v_max3_f32 v248, v0, v1, v2\n\t"
    "v_max3_f32 v248, v248, v3, v4\n\t"
    "v_mfma_f32_32x32x16_bf16 a[16:31], v[128:131], v[80:83], a[16:31]\n\t"
    "ds_read_b128 a[208:211], v228 offset:16384\n\t"
    "ds_read_b128 a[212:215], v229 offset:16384\n\t"
    "ds_read_b128 a[216:219], v230 offset:16384\n\t"
    "ds_read_b128 a[220:223], v231 offset:16384\n\t"
    "v_max3_f32 v248, v248, v5, v6\n\t"
    "v_max3_f32 v248, v248, v7, v8\n\t"
    "v_mfma_f32_32x32x16_bf16 a[32:47], v[144:147], v[64:67], a[32:47]\n\t"
    "ds_read_b128 a[224:227], v224 offset:24576\n\t"
    "ds_read_b128 a[228:231], v225 offset:24576\n\t"
    "ds_read_b128 a[232:235], v226 offset:24576\n\t"
    "ds_read_b128 a[236:239], v227 offset:24576\n\t"
    "v_max3_f32 v248, v248, v9, v10\n\t"
    "v_max3_f32 v248, v248, v11, v12\n\t"
    "v_mfma_f32_32x32x16_bf16 a[48:63], v[144:147], v[80:83], a[48:63]\n\t"
    "ds_read_b128 a[240:243], v228 offset:24576\n\t"
    "ds_read_b128 a[244:247], v229 offset:24576\n\t"
    "ds_read_b128 a[248:251], v230 offset:24576\n\t"
    "ds_read_b128 a[252:255], v231 offset:24576\n\t"
    "v_max3_f32 v248, v248, v13, v14\n\t"
    "v_max3_f32 v248, v248, v15, v32\n\t"
    "v_mfma_f32_32x32x16_bf16 a[64:79], v[160:163], v[64:67], a[64:79]\n\t"
    "v_max3_f32 v248, v248, v33, v34\n\t"
    "v_max3_f32 v248, v248, v35, v36\n\t"
    "v_max3_f32 v248, v248, v37, v38\n\t"
    "v_max3_f32 v248, v248, v39, v40\n\t"
    "v_max3_f32 v248, v248, v41, v42\n\t"
    "v_max3_f32 v248, v248, v43, v44\n\t"
    "v_mfma_f32_32x32x16_bf16 a[80:95], v[160:163], v[80:83], a[80:95]\n\t"
    "v_max3_f32 v248, v248, v45, v46\n\t"
    "v_max_f32 v248, v248, v47\n\t"
    "v_max3_f32 v249, v16, v17, v18\n\t"
    "v_max3_f32 v249, v249, v19, v20\n\t"
    "v_max3_f32 v249, v249, v21, v22\n\t"
    "v_max3_f32 v249, v249, v23, v24\n\t"
    "v_mfma_f32_32x32x16_bf16 a[96:111], v[176:179], v[64:67], a[96:111]\n\t"
    "v_max3_f32 v249, v249, v25, v26\n\t"
    "v_max3_f32 v249, v249, v27, v28\n\t"
    "v_max3_f32 v249, v249, v29, v30\n\t"
    "v_max3_f32 v249, v249, v31, v48\n\t"
    "v_max3_f32 v249, v249, v49, v50\n\t"
    "v_max3_f32 v249, v249, v51, v52\n\t"
    "v_mfma_f32_32x32x16_bf16 a[112:127], v[176:179], v[80:83], a[112:127]\n\t"
    "v_max3_f32 v249, v249, v53, v54\n\t"
    "v_max3_f32 v249, v249, v55, v56\n\t"
    "v_max3_f32 v249, v249, v57, v58\n\t"
    "v_max3_f32 v249, v249, v59, v60\n\t"
    "v_max3_f32 v249, v249, v61, v62\n\t"
    "v_max_f32 v249, v249, v63\n\t"
    "v_mfma_f32_32x32x16_bf16 a[0:15], v[132:135], v[72:75], a[0:15]\n\t"
    "v_max_f32 v252, v248, v249\n\t"
    "v_cmp_lt_f32 vcc, s44, v252\n\t"
    "s_nop 1\n\t"
    "s_cbranch_vccnz RARE1_4%=\n\t"
    "RARE1R_4%=:\n\t"
    "v_mfma_f32_32x32x16_bf16 a[16:31], v[132:135], v[88:91], a[16:31]\n\t"
    "v_exp_f32 v0, v0\n\t"
    "v_exp_f32 v1, v1\n\t"
    "v_mfma_f32_32x32x16_bf16 a[32:47], v[148:151], v[72:75], a[32:47]\n\t"
    "v_exp_f32 v2, v2\n\t"
    "v_exp_f32 v3, v3\n\t"
    "v_add_f32 v244, v244, v0\n\t"
    "v_add_f32 v245, v245, v1\n\t"
    "v_mfma_f32_32x32x16_bf16 a[48:63], v[148:151], v[88:91], a[48:63]\n\t"
    "v_exp_f32 v4, v4\n\t"
    "v_exp_f32 v5, v5\n\t"
    "v_add_f32 v244, v244, v2\n\t"
    "v_add_f32 v245, v245, v3\n\t"
    "v_mfma_f32_32x32x16_bf16 a[64:79], v[164:167], v[72:75], a[64:79]\n\t"
    "v_exp_f32 v6, v6\n\t"
    "v_exp_f32 v7, v7\n\t"
    "v_add_f32 v244, v244, v4\n\t"
    "v_add_f32 v245, v245, v5\n\t"
    "v_mfma_f32_32x32x16_bf16 a[80:95], v[164:167], v[88:91], a[80:95]\n\t"
    "v_exp_f32 v8, v8\n\t"
    "v_exp_f32 v9, v9\n\t"
    "v_add_f32 v244, v244, v6\n\t"
    "v_add_f32 v245, v245, v7\n\t"
    "v_mfma_f32_32x32x16_bf16 a[96:111], v[180:183], v[72:75], a[96:111]\n\t"
    "v_exp_f32 v10, v10\n\t"
    "v_exp_f32 v11, v11\n\t"
    "v_add_f32 v244, v244, v8\n\t"
    "v_add_f32 v245, v245, v9\n\t"
    "v_mfma_f32_32x32x16_bf16 a[112:127], v[180:183], v[88:91], a[112:127]\n\t"
    "v_exp_f32 v12, v12\n\t"
    "v_exp_f32 v13, v13\n\t"
    "v_add_f32 v244, v244, v10\n\t"
    "v_add_f32 v245, v245, v11\n\t"
    "v_mfma_f32_32x32x16_bf16 a[0:15], v[136:139], v[96:99], a[0:15]\n\t"
    "v_exp_f32 v14, v14\n\t"
    "v_exp_f32 v15, v15\n\t"
    "v_add_f32 v244, v244, v12\n\t"
    "v_add_f32 v245, v245, v13\n\t"
    "v_mfma_f32_32x32x16_bf16 a[16:31], v[136:139], v[112:115], a[16:31]\n\t"
    "v_exp_f32 v16, v16\n\t"
    "v_exp_f32 v17, v17\n\t"
    "v_add_f32 v244, v244, v14\n\t"
    "v_add_f32 v245, v245, v15\n\t"
    "v_mfma_f32_32x32x16_bf16 a[32:47], v[152:155], v[96:99], a[32:47]\n\t"
    "v_exp_f32 v18, v18\n\t"
    "v_exp_f32 v19, v19\n\t"
    "v_add_f32 v246, v246, v16\n\t"
    "v_add_f32 v247, v247, v17\n\t"
    "v_mfma_f32_32x32x16_bf16 a[48:63], v[152:155], v[112:115], a[48:63]\n\t"
    "v_exp_f32 v20, v20\n\t"
    "v_exp_f32 v21, v21\n\t"
    "v_add_f32 v246, v246, v18\n\t"
    "v_add_f32 v247, v247, v19\n\t"
    "v_mfma_f32_32x32x16_bf16 a[64:79], v[168:171], v[96:99], a[64:79]\n\t"
    "v_exp_f32 v22, v22\n\t"
    "v_exp_f32 v23, v23\n\t"
    "v_add_f32 v246, v246, v20\n\t"
    "v_add_f32 v247, v247, v21\n\t"
    "v_mfma_f32_32x32x16_bf16 a[80:95], v[168:171], v[112:115], a[80:95]\n\t"
    "v_exp_f32 v24, v24\n\t"
    "v_exp_f32 v25, v25\n\t"
    "v_add_f32 v246, v246, v22\n\t"
    "v_add_f32 v247, v247, v23\n\t"
    "v_mfma_f32_32x32x16_bf16 a[96:111], v[184:187], v[96:99], a[96:111]\n\t"
    "v_exp_f32 v26, v26\n\t"
    "v_exp_f32 v27, v27\n\t"
    "v_add_f32 v246, v246, v24\n\t"
    "v_add_f32 v247, v247, v25\n\t"
    "v_mfma_f32_32x32x16_bf16 a[112:127], v[184:187], v[112:115], a[112:127]\n\t"
    "v_exp_f32 v28, v28\n\t"
    "v_exp_f32 v29, v29\n\t"
    "v_add_f32 v246, v246, v26\n\t"
    "v_add_f32 v247, v247, v27\n\t"
    "v_mfma_f32_32x32x16_bf16 a[0:15], v[140:143], v[104:107], a[0:15]\n\t"
    "v_exp_f32 v30, v30\n\t"
    "v_exp_f32 v31, v31\n\t"
    "v_add_f32 v246, v246, v28\n\t"
    "v_add_f32 v247, v247, v29\n\t"
    "v_mfma_f32_32x32x16_bf16 a[16:31], v[140:143], v[120:123], a[16:31]\n\t"
    "v_exp_f32 v32, v32\n\t"
    "v_exp_f32 v33, v33\n\t"
    "v_add_f32 v246, v246, v30\n\t"
    "v_add_f32 v247, v247, v31\n\t"
    "v_mfma_f32_32x32x16_bf16 a[32:47], v[156:159], v[104:107], a[32:47]\n\t"
    "v_exp_f32 v34, v34\n\t"
    "v_exp_f32 v35, v35\n\t"
    "v_add_f32 v244, v244, v32\n\t"
    "v_add_f32 v245, v245, v33\n\t"
    "v_mfma_f32_32x32x16_bf16 a[48:63], v[156:159], v[120:123], a[48:63]\n\t"
    "v_exp_f32 v36, v36\n\t"
    "v_exp_f32 v37, v37\n\t"
    "v_add_f32 v244, v244, v34\n\t"
    "v_add_f32 v245, v245, v35\n\t"
    "v_mfma_f32_32x32x16_bf16 a[64:79], v[172:175], v[104:107], a[64:79]\n\t"
    "v_exp_f32 v38, v38\n\t"
    "v_exp_f32 v39, v39\n\t"
    "v_add_f32 v244, v244, v36\n\t"
    "v_add_f32 v245, v245, v37\n\t"
    "v_mfma_f32_32x32x16_bf16 a[80:95], v[172:175], v[120:123], a[80:95]\n\t"
    "v_add_f32 v244, v244, v38\n\t"
    "v_add_f32 v245, v245, v39\n\t"
    "v_mfma_f32_32x32x16_bf16 a[96:111], v[188:191], v[104:107], a[96:111]\n\t"
    "v_mfma_f32_32x32x16_bf16 a[112:127], v[188:191], v[120:123], a[112:127]\n\t"
    "s_cmp_lg_u32 s46, 0\n\t"
    "s_cbranch_scc1 RARE2_4%=\n\t"
    "RARE2R_4%=:\n\t"
    "s_waitcnt lgkmcnt(0)\n\t"
    "s_branch LOOP%=\n\t"
    "TAIL%=:\n\t"
    "s_waitcnt lgkmcnt(0)\n\t"
    "v_mfma_f32_32x32x16_bf16 a[0:15], v[128:131], v[64:67], a[0:15]\n\t"
    "v_mfma_f32_32x32x16_bf16 a[16:31], v[128:131], v[80:83], a[16:31]\n\t"
    "v_mfma_f32_32x32x16_bf16 a[32:47], v[144:147], v[64:67], a[32:47]\n\t"
    "v_mfma_f32_32x32x16_bf16 a[48:63], v[144:147], v[80:83], a[48:63]\n\t"
    "v_mfma_f32_32x32x16_bf16 a[64:79], v[160:163], v[64:67], a[64:79]\n\t"
    "v_mfma_f32_32x32x16_bf16 a[80:95], v[160:163], v[80:83], a[80:95]\n\t"
    "v_mfma_f32_32x32x16_bf16 a[96:111], v[176:179], v[64:67], a[96:111]\n\t"
    "v_mfma_f32_32x32x16_bf16 a[112:127], v[176:179], v[80:83], a[112:127]\n\t"
    "v_mfma_f32_32x32x16_bf16 a[0:15], v[132:135], v[72:75], a[0:15]\n\t"
    "v_mfma_f32_32x32x16_bf16 a[16:31], v[132:135], v[88:91], a[16:31]\n\t"
    "v_mfma_f32_32x32x16_bf16 a[32:47], v[148:151], v[72:75], a[32:47]\n\t"
    "v_mfma_f32_32x32x16_bf16 a[48:63], v[148:151], v[88:91], a[48:63]\n\t"
    "v_mfma_f32_32x32x16_bf16 a[64:79], v[164:167], v[72:75], a[64:79]\n\t"
    "v_mfma_f32_32x32x16_bf16 a[80:95], v[164:167], v[88:91], a[80:95]\n\t"
    "v_mfma_f32_32x32x16_bf16 a[96:111], v[180:183], v[72:75], a[96:111]\n\t"
    "v_mfma_f32_32x32x16_bf16 a[112:127], v[180:183], v[88:91], a[112:127]\n\t"
    "v_mfma_f32_32x32x16_bf16 a[0:15], v[136:139], v[96:99], a[0:15]\n\t"
    "v_mfma_f32_32x32x16_bf16 a[16:31], v[136:139], v[112:115], a[16:31]\n\t"
    "v_mfma_f32_32x32x16_bf16 a[32:47], v[152:155], v[96:99], a[32:47]\n\t"
    "v_mfma_f32_32x32x16_bf16 a[48:63], v[152:155], v[112:115], a[48:63]\n\t"
    "v_mfma_f32_32x32x16_bf16 a[64:79], v[168:171], v[96:99], a[64:79]\n\t"
    "v_mfma_f32_32x32x16_bf16 a[80:95], v[168:171], v[112:115], a[80:95]\n\t"
    "v_mfma_f32_32x32x16_bf16 a[96:111], v[184:187], v[96:99], a[96:111]\n\t"
    "v_mfma_f32_32x32x16_bf16 a[112:127], v[184:187], v[112:115], a[112:127]\n\t"
    "v_mfma_f32_32x32x16_bf16 a[0:15], v[140:143], v[104:107], a[0:15]\n\t"
    "v_mfma_f32_32x32x16_bf16 a[16:31], v[140:143], v[120:123], a[16:31]\n\t"
    "v_mfma_f32_32x32x16_bf16 a[32:47], v[156:159], v[104:107], a[32:47]\n\t"
    "v_mfma_f32_32x32x16_bf16 a[48:63], v[156:159], v[120:123], a[48:63]\n\t"
    "v_mfma_f32_32x32x16_bf16 a[64:79], v[172:175], v[104:107], a[64:79]\n\t"
    "v_mfma_f32_32x32x16_bf16 a[80:95], v[172:175], v[120:123], a[80:95]\n\t"
    "v_mfma_f32_32x32x16_bf16 a[96:111], v[188:191], v[104:107], a[96:111]\n\t"
    "v_mfma_f32_32x32x16_bf16 a[112:127], v[188:191], v[120:123], a[112:127]\n\t"
    "v_add_f32 v244, v244, v245\n\t"
    "v_mov_b32 v252, v244\n\t"
    "s_nop 1\n\t"
    "v_permlane32_swap_b32 v244, v252\n\t"
    "v_add_f32 v244, v244, v252\n\t"
    "v_rcp_f32 v244, v244\n\t"
    "v_add_f32 v246, v246, v247\n\t"
    "v_mov_b32 v252, v246\n\t"
    "s_nop 1\n\t"
    "v_permlane32_swap_b32 v246, v252\n\t"
    "v_add_f32 v246, v246, v252\n\t"
    "v_rcp_f32 v246, v246\n\t"
    "v_accvgpr_read_b32 v0, a0\n\t"
    "v_accvgpr_read_b32 v1, a1\n\t"
    "v_accvgpr_read_b32 v2, a2\n\t"
    "v_accvgpr_read_b32 v3, a3\n\t"
    "v_accvgpr_read_b32 v4, a4\n\t"
    "v_accvgpr_read_b32 v5, a5\n\t"
    "v_accvgpr_read_b32 v6, a6\n\t"
    "v_accvgpr_read_b32 v7, a7\n\t"
    "v_accvgpr_read_b32 v8, a8\n\t"
    "v_accvgpr_read_b32 v9, a9\n\t"
    "v_accvgpr_read_b32 v10, a10\n\t"
    "v_accvgpr_read_b32 v11, a11\n\t"
    "v_accvgpr_read_b32 v12, a12\n\t"
    "v_accvgpr_read_b32 v13, a13\n\t"
    "v_accvgpr_read_b32 v14, a14\n\t"
    "v_accvgpr_read_b32 v15, a15\n\t"
    "v_mul_f32 v0, v0, v244\n\t"
    "v_mul_f32 v1, v1, v244\n\t"
    "v_mul_f32 v2, v2, v244\n\t"
    "v_mul_f32 v3, v3, v244\n\t"
    "v_mul_f32 v4, v4, v244\n\t"
    "v_mul_f32 v5, v5, v244\n\t"
    "v_mul_f32 v6, v6, v244\n\t"
    "v_mul_f32 v7, v7, v244\n\t"
    "v_mul_f32 v8, v8, v244\n\t"
    "v_mul_f32 v9, v9, v244\n\t"
    "v_mul_f32 v10, v10, v244\n\t"
    "v_mul_f32 v11, v11, v244\n\t"
    "v_mul_f32 v12, v12, v244\n\t"
    "v_mul_f32 v13, v13, v244\n\t"
    "v_mul_f32 v14, v14, v244\n\t"
    "v_mul_f32 v15, v15, v244\n\t"
    "v_cvt_pk_bf16_f32 v0, v0, v1\n\t"
    "v_cvt_pk_bf16_f32 v1, v2, v3\n\t"
    "v_cvt_pk_bf16_f32 v2, v4, v5\n\t"
    "v_cvt_pk_bf16_f32 v3, v6, v7\n\t"
    "v_cvt_pk_bf16_f32 v4, v8, v9\n\t"
    "v_cvt_pk_bf16_f32 v5, v10, v11\n\t"
    "v_cvt_pk_bf16_f32 v6, v12, v13\n\t"
    "v_cvt_pk_bf16_f32 v7, v14, v15\n\t"
    "v_permlane32_swap_b32 v0, v2\n\t"
    "v_permlane32_swap_b32 v1, v3\n\t"
    "v_permlane32_swap_b32 v4, v6\n\t"
    "v_permlane32_swap_b32 v5, v7\n\t"
    "buffer_store_dwordx4 v[0:3], v241, %3, 0 offen offset:0\n\t"
    "buffer_store_dwordx4 v[4:7], v241, %3, 0 offen offset:32\n\t"
    "v_accvgpr_read_b32 v16, a32\n\t"
    "v_accvgpr_read_b32 v17, a33\n\t"
    "v_accvgpr_read_b32 v18, a34\n\t"
    "v_accvgpr_read_b32 v19, a35\n\t"
    "v_accvgpr_read_b32 v20, a36\n\t"
    "v_accvgpr_read_b32 v21, a37\n\t"
    "v_accvgpr_read_b32 v22, a38\n\t"
    "v_accvgpr_read_b32 v23, a39\n\t"
    "v_accvgpr_read_b32 v24, a40\n\t"
    "v_accvgpr_read_b32 v25, a41\n\t"
    "v_accvgpr_read_b32 v26, a42\n\t"
    "v_accvgpr_read_b32 v27, a43\n\t"
    "v_accvgpr_read_b32 v28, a44\n\t"
    "v_accvgpr_read_b32 v29, a45\n\t"
    "v_accvgpr_read_b32 v30, a46\n\t"
    "v_accvgpr_read_b32 v31, a47\n\t"
    "v_mul_f32 v16, v16, v244\n\t"
    "v_mul_f32 v17, v17, v244\n\t"
    "v_mul_f32 v18, v18, v244\n\t"
    "v_mul_f32 v19, v19, v244\n\t"
    "v_mul_f32 v20, v20, v244\n\t"
    "v_mul_f32 v21, v21, v244\n\t"
    "v_mul_f32 v22, v22, v244\n\t"
    "v_mul_f32 v23, v23, v244\n\t"
    "v_mul_f32 v24, v24, v244\n\t"
    "v_mul_f32 v25, v25, v244\n\t"
    "v_mul_f32 v26, v26, v244\n\t"
    "v_mul_f32 v27, v27, v244\n\t"
    "v_mul_f32 v28, v28, v244\n\t"
    "v_mul_f32 v29, v29, v244\n\t"
    "v_mul_f32 v30, v30, v244\n\t"
    "v_mul_f32 v31, v31, v244\n\t"
    "v_cvt_pk_bf16_f32 v16, v16, v17\n\t"
    "v_cvt_pk_bf16_f32 v17, v18, v19\n\t"
    "v_cvt_pk_bf16_f32 v18, v20, v21\n\t"
    "v_cvt_pk_bf16_f32 v19, v22, v23\n\t"
    "v_cvt_pk_bf16_f32 v20, v24, v25\n\t"
    "v_cvt_pk_bf16_f32 v21, v26, v27\n\t"
    "v_cvt_pk_bf16_f32 v22, v28, v29\n\t"
    "v_cvt_pk_bf16_f32 v23, v30, v31\n\t"
    "v_permlane32_swap_b32 v16, v18\n\t"
    "v_permlane32_swap_b32 v17, v19\n\t"
    "v_permlane32_swap_b32 v20, v22\n\t"
    "v_permlane32_swap_b32 v21, v23\n\t"
    "buffer_store_dwordx4 v[16:19], v241, %3, 0 offen offset:64\n\t"
    "buffer_store_dwordx4 v[20:23], v241, %3, 0 offen offset:96\n\t"
    "v_accvgpr_read_b32 v32, a64\n\t"
    "v_accvgpr_read_b32 v33, a65\n\t"
    "v_accvgpr_read_b32 v34, a66\n\t"
    "v_accvgpr_read_b32 v35, a67\n\t"
    "v_accvgpr_read_b32 v36, a68\n\t"
    "v_accvgpr_read_b32 v37, a69\n\t"
    "v_accvgpr_read_b32 v38, a70\n\t"
    "v_accvgpr_read_b32 v39, a71\n\t"
    "v_accvgpr_read_b32 v40, a72\n\t"
    "v_accvgpr_read_b32 v41, a73\n\t"
    "v_accvgpr_read_b32 v42, a74\n\t"
    "v_accvgpr_read_b32 v43, a75\n\t"
    "v_accvgpr_read_b32 v44, a76\n\t"
    "v_accvgpr_read_b32 v45, a77\n\t"
    "v_accvgpr_read_b32 v46, a78\n\t"
    "v_accvgpr_read_b32 v47, a79\n\t"
    "v_mul_f32 v32, v32, v244\n\t"
    "v_mul_f32 v33, v33, v244\n\t"
    "v_mul_f32 v34, v34, v244\n\t"
    "v_mul_f32 v35, v35, v244\n\t"
    "v_mul_f32 v36, v36, v244\n\t"
    "v_mul_f32 v37, v37, v244\n\t"
    "v_mul_f32 v38, v38, v244\n\t"
    "v_mul_f32 v39, v39, v244\n\t"
    "v_mul_f32 v40, v40, v244\n\t"
    "v_mul_f32 v41, v41, v244\n\t"
    "v_mul_f32 v42, v42, v244\n\t"
    "v_mul_f32 v43, v43, v244\n\t"
    "v_mul_f32 v44, v44, v244\n\t"
    "v_mul_f32 v45, v45, v244\n\t"
    "v_mul_f32 v46, v46, v244\n\t"
    "v_mul_f32 v47, v47, v244\n\t"
    "v_cvt_pk_bf16_f32 v32, v32, v33\n\t"
    "v_cvt_pk_bf16_f32 v33, v34, v35\n\t"
    "v_cvt_pk_bf16_f32 v34, v36, v37\n\t"
    "v_cvt_pk_bf16_f32 v35, v38, v39\n\t"
    "v_cvt_pk_bf16_f32 v36, v40, v41\n\t"
    "v_cvt_pk_bf16_f32 v37, v42, v43\n\t"
    "v_cvt_pk_bf16_f32 v38, v44, v45\n\t"
    "v_cvt_pk_bf16_f32 v39, v46, v47\n\t"
    "v_permlane32_swap_b32 v32, v34\n\t"
    "v_permlane32_swap_b32 v33, v35\n\t"
    "v_permlane32_swap_b32 v36, v38\n\t"
    "v_permlane32_swap_b32 v37, v39\n\t"
    "buffer_store_dwordx4 v[32:35], v241, %3, 0 offen offset:128\n\t"
    "buffer_store_dwordx4 v[36:39], v241, %3, 0 offen offset:160\n\t"
    "v_accvgpr_read_b32 v48, a96\n\t"
    "v_accvgpr_read_b32 v49, a97\n\t"
    "v_accvgpr_read_b32 v50, a98\n\t"
    "v_accvgpr_read_b32 v51, a99\n\t"
    "v_accvgpr_read_b32 v52, a100\n\t"
    "v_accvgpr_read_b32 v53, a101\n\t"
    "v_accvgpr_read_b32 v54, a102\n\t"
    "v_accvgpr_read_b32 v55, a103\n\t"
    "v_accvgpr_read_b32 v56, a104\n\t"
    "v_accvgpr_read_b32 v57, a105\n\t"
    "v_accvgpr_read_b32 v58, a106\n\t"
    "v_accvgpr_read_b32 v59, a107\n\t"
    "v_accvgpr_read_b32 v60, a108\n\t"
    "v_accvgpr_read_b32 v61, a109\n\t"
    "v_accvgpr_read_b32 v62, a110\n\t"
    "v_accvgpr_read_b32 v63, a111\n\t"
    "v_mul_f32 v48, v48, v244\n\t"
    "v_mul_f32 v49, v49, v244\n\t"
    "v_mul_f32 v50, v50, v244\n\t"
    "v_mul_f32 v51, v51, v244\n\t"
    "v_mul_f32 v52, v52, v244\n\t"
    "v_mul_f32 v53, v53, v244\n\t"
    "v_mul_f32 v54, v54, v244\n\t"
    "v_mul_f32 v55, v55, v244\n\t"
    "v_mul_f32 v56, v56, v244\n\t"
    "v_mul_f32 v57, v57, v244\n\t"
    "v_mul_f32 v58, v58, v244\n\t"
    "v_mul_f32 v59, v59, v244\n\t"
    "v_mul_f32 v60, v60, v244\n\t"
    "v_mul_f32 v61, v61, v244\n\t"
    "v_mul_f32 v62, v62, v244\n\t"
    "v_mul_f32 v63, v63, v244\n\t"
    "v_cvt_pk_bf16_f32 v48, v48, v49\n\t"
    "v_cvt_pk_bf16_f32 v49, v50, v51\n\t"
    "v_cvt_pk_bf16_f32 v50, v52, v53\n\t"
    "v_cvt_pk_bf16_f32 v51, v54, v55\n\t"
    "v_cvt_pk_bf16_f32 v52, v56, v57\n\t"
    "v_cvt_pk_bf16_f32 v53, v58, v59\n\t"
    "v_cvt_pk_bf16_f32 v54, v60, v61\n\t"
    "v_cvt_pk_bf16_f32 v55, v62, v63\n\t"
    "v_permlane32_swap_b32 v48, v50\n\t"
    "v_permlane32_swap_b32 v49, v51\n\t"
    "v_permlane32_swap_b32 v52, v54\n\t"
    "v_permlane32_swap_b32 v53, v55\n\t"
    "buffer_store_dwordx4 v[48:51], v241, %3, 0 offen offset:192\n\t"
    "buffer_store_dwordx4 v[52:55], v241, %3, 0 offen offset:224\n\t"
    "v_accvgpr_read_b32 v64, a16\n\t"
    "v_accvgpr_read_b32 v65, a17\n\t"
    "v_accvgpr_read_b32 v66, a18\n\t"
    "v_accvgpr_read_b32 v67, a19\n\t"
    "v_accvgpr_read_b32 v68, a20\n\t"
    "v_accvgpr_read_b32 v69, a21\n\t"
    "v_accvgpr_read_b32 v70, a22\n\t"
    "v_accvgpr_read_b32 v71, a23\n\t"
    "v_accvgpr_read_b32 v72, a24\n\t"
    "v_accvgpr_read_b32 v73, a25\n\t"
    "v_accvgpr_read_b32 v74, a26\n\t"
    "v_accvgpr_read_b32 v75, a27\n\t"
    "v_accvgpr_read_b32 v76, a28\n\t"
    "v_accvgpr_read_b32 v77, a29\n\t"
    "v_accvgpr_read_b32 v78, a30\n\t"
    "v_accvgpr_read_b32 v79, a31\n\t"
    "v_mul_f32 v64, v64, v246\n\t"
    "v_mul_f32 v65, v65, v246\n\t"
    "v_mul_f32 v66, v66, v246\n\t"
    "v_mul_f32 v67, v67, v246\n\t"
    "v_mul_f32 v68, v68, v246\n\t"
    "v_mul_f32 v69, v69, v246\n\t"
    "v_mul_f32 v70, v70, v246\n\t"
    "v_mul_f32 v71, v71, v246\n\t"
    "v_mul_f32 v72, v72, v246\n\t"
    "v_mul_f32 v73, v73, v246\n\t"
    "v_mul_f32 v74, v74, v246\n\t"
    "v_mul_f32 v75, v75, v246\n\t"
    "v_mul_f32 v76, v76, v246\n\t"
    "v_mul_f32 v77, v77, v246\n\t"
    "v_mul_f32 v78, v78, v246\n\t"
    "v_mul_f32 v79, v79, v246\n\t"
    "v_cvt_pk_bf16_f32 v64, v64, v65\n\t"
    "v_cvt_pk_bf16_f32 v65, v66, v67\n\t"
    "v_cvt_pk_bf16_f32 v66, v68, v69\n\t"
    "v_cvt_pk_bf16_f32 v67, v70, v71\n\t"
    "v_cvt_pk_bf16_f32 v68, v72, v73\n\t"
    "v_cvt_pk_bf16_f32 v69, v74, v75\n\t"
    "v_cvt_pk_bf16_f32 v70, v76, v77\n\t"
    "v_cvt_pk_bf16_f32 v71, v78, v79\n\t"
    "v_permlane32_swap_b32 v64, v66\n\t"
    "v_permlane32_swap_b32 v65, v67\n\t"
    "v_permlane32_swap_b32 v68, v70\n\t"
    "v_permlane32_swap_b32 v69, v71\n\t"
    "buffer_store_dwordx4 v[64:67], v241, %3, s45 offen offset:0\n\t"
    "buffer_store_dwordx4 v[68:71], v241, %3, s45 offen offset:32\n\t"
    "v_accvgpr_read_b32 v80, a48\n\t"
    "v_accvgpr_read_b32 v81, a49\n\t"
    "v_accvgpr_read_b32 v82, a50\n\t"
    "v_accvgpr_read_b32 v83, a51\n\t"
    "v_accvgpr_read_b32 v84, a52\n\t"
    "v_accvgpr_read_b32 v85, a53\n\t"
    "v_accvgpr_read_b32 v86, a54\n\t"
    "v_accvgpr_read_b32 v87, a55\n\t"
    "v_accvgpr_read_b32 v88, a56\n\t"
    "v_accvgpr_read_b32 v89, a57\n\t"
    "v_accvgpr_read_b32 v90, a58\n\t"
    "v_accvgpr_read_b32 v91, a59\n\t"
    "v_accvgpr_read_b32 v92, a60\n\t"
    "v_accvgpr_read_b32 v93, a61\n\t"
    "v_accvgpr_read_b32 v94, a62\n\t"
    "v_accvgpr_read_b32 v95, a63\n\t"
    "v_mul_f32 v80, v80, v246\n\t"
    "v_mul_f32 v81, v81, v246\n\t"
    "v_mul_f32 v82, v82, v246\n\t"
    "v_mul_f32 v83, v83, v246\n\t"
    "v_mul_f32 v84, v84, v246\n\t"
    "v_mul_f32 v85, v85, v246\n\t"
    "v_mul_f32 v86, v86, v246\n\t"
    "v_mul_f32 v87, v87, v246\n\t"
    "v_mul_f32 v88, v88, v246\n\t"
    "v_mul_f32 v89, v89, v246\n\t"
    "v_mul_f32 v90, v90, v246\n\t"
    "v_mul_f32 v91, v91, v246\n\t"
    "v_mul_f32 v92, v92, v246\n\t"
    "v_mul_f32 v93, v93, v246\n\t"
    "v_mul_f32 v94, v94, v246\n\t"
    "v_mul_f32 v95, v95, v246\n\t"
    "v_cvt_pk_bf16_f32 v80, v80, v81\n\t"
    "v_cvt_pk_bf16_f32 v81, v82, v83\n\t"
    "v_cvt_pk_bf16_f32 v82, v84, v85\n\t"
    "v_cvt_pk_bf16_f32 v83, v86, v87\n\t"
    "v_cvt_pk_bf16_f32 v84, v88, v89\n\t"
    "v_cvt_pk_bf16_f32 v85, v90, v91\n\t"
    "v_cvt_pk_bf16_f32 v86, v92, v93\n\t"
    "v_cvt_pk_bf16_f32 v87, v94, v95\n\t"
    "v_permlane32_swap_b32 v80, v82\n\t"
    "v_permlane32_swap_b32 v81, v83\n\t"
    "v_permlane32_swap_b32 v84, v86\n\t"
    "v_permlane32_swap_b32 v85, v87\n\t"
    "buffer_store_dwordx4 v[80:83], v241, %3, s45 offen offset:64\n\t"
    "buffer_store_dwordx4 v[84:87], v241, %3, s45 offen offset:96\n\t"
    "v_accvgpr_read_b32 v96, a80\n\t"
    "v_accvgpr_read_b32 v97, a81\n\t"
    "v_accvgpr_read_b32 v98, a82\n\t"
    "v_accvgpr_read_b32 v99, a83\n\t"
    "v_accvgpr_read_b32 v100, a84\n\t"
    "v_accvgpr_read_b32 v101, a85\n\t"
    "v_accvgpr_read_b32 v102, a86\n\t"
    "v_accvgpr_read_b32 v103, a87\n\t"
    "v_accvgpr_read_b32 v104, a88\n\t"
    "v_accvgpr_read_b32 v105, a89\n\t"
    "v_accvgpr_read_b32 v106, a90\n\t"
    "v_accvgpr_read_b32 v107, a91\n\t"
    "v_accvgpr_read_b32 v108, a92\n\t"
    "v_accvgpr_read_b32 v109, a93\n\t"
    "v_accvgpr_read_b32 v110, a94\n\t"
    "v_accvgpr_read_b32 v111, a95\n\t"
    "v_mul_f32 v96, v96, v246\n\t"
    "v_mul_f32 v97, v97, v246\n\t"
    "v_mul_f32 v98, v98, v246\n\t"
    "v_mul_f32 v99, v99, v246\n\t"
    "v_mul_f32 v100, v100, v246\n\t"
    "v_mul_f32 v101, v101, v246\n\t"
    "v_mul_f32 v102, v102, v246\n\t"
    "v_mul_f32 v103, v103, v246\n\t"
    "v_mul_f32 v104, v104, v246\n\t"
    "v_mul_f32 v105, v105, v246\n\t"
    "v_mul_f32 v106, v106, v246\n\t"
    "v_mul_f32 v107, v107, v246\n\t"
    "v_mul_f32 v108, v108, v246\n\t"
    "v_mul_f32 v109, v109, v246\n\t"
    "v_mul_f32 v110, v110, v246\n\t"
    "v_mul_f32 v111, v111, v246\n\t"
    "v_cvt_pk_bf16_f32 v96, v96, v97\n\t"
    "v_cvt_pk_bf16_f32 v97, v98, v99\n\t"
    "v_cvt_pk_bf16_f32 v98, v100, v101\n\t"
    "v_cvt_pk_bf16_f32 v99, v102, v103\n\t"
    "v_cvt_pk_bf16_f32 v100, v104, v105\n\t"
    "v_cvt_pk_bf16_f32 v101, v106, v107\n\t"
    "v_cvt_pk_bf16_f32 v102, v108, v109\n\t"
    "v_cvt_pk_bf16_f32 v103, v110, v111\n\t"
    "v_permlane32_swap_b32 v96, v98\n\t"
    "v_permlane32_swap_b32 v97, v99\n\t"
    "v_permlane32_swap_b32 v100, v102\n\t"
    "v_permlane32_swap_b32 v101, v103\n\t"
    "buffer_store_dwordx4 v[96:99], v241, %3, s45 offen offset:128\n\t"
    "buffer_store_dwordx4 v[100:103], v241, %3, s45 offen offset:160\n\t"
    "v_accvgpr_read_b32 v112, a112\n\t"
    "v_accvgpr_read_b32 v113, a113\n\t"
    "v_accvgpr_read_b32 v114, a114\n\t"
    "v_accvgpr_read_b32 v115, a115\n\t"
    "v_accvgpr_read_b32 v116, a116\n\t"
    "v_accvgpr_read_b32 v117, a117\n\t"
    "v_accvgpr_read_b32 v118, a118\n\t"
    "v_accvgpr_read_b32 v119, a119\n\t"
    "v_accvgpr_read_b32 v120, a120\n\t"
    "v_accvgpr_read_b32 v121, a121\n\t"
    "v_accvgpr_read_b32 v122, a122\n\t"
    "v_accvgpr_read_b32 v123, a123\n\t"
    "v_accvgpr_read_b32 v124, a124\n\t"
    "v_accvgpr_read_b32 v125, a125\n\t"
    "v_accvgpr_read_b32 v126, a126\n\t"
    "v_accvgpr_read_b32 v127, a127\n\t"
    "v_mul_f32 v112, v112, v246\n\t"
    "v_mul_f32 v113, v113, v246\n\t"
    "v_mul_f32 v114, v114, v246\n\t"
    "v_mul_f32 v115, v115, v246\n\t"
    "v_mul_f32 v116, v116, v246\n\t"
    "v_mul_f32 v117, v117, v246\n\t"
    "v_mul_f32 v118, v118, v246\n\t"
    "v_mul_f32 v119, v119, v246\n\t"
    "v_mul_f32 v120, v120, v246\n\t"
    "v_mul_f32 v121, v121, v246\n\t"
    "v_mul_f32 v122, v122, v246\n\t"
    "v_mul_f32 v123, v123, v246\n\t"
    "v_mul_f32 v124, v124, v246\n\t"
    "v_mul_f32 v125, v125, v246\n\t"
    "v_mul_f32 v126, v126, v246\n\t"
    "v_mul_f32 v127, v127, v246\n\t"
    "v_cvt_pk_bf16_f32 v112, v112, v113\n\t"
    "v_cvt_pk_bf16_f32 v113, v114, v115\n\t"
    "v_cvt_pk_bf16_f32 v114, v116, v117\n\t"
    "v_cvt_pk_bf16_f32 v115, v118, v119\n\t"
    "v_cvt_pk_bf16_f32 v116, v120, v121\n\t"
    "v_cvt_pk_bf16_f32 v117, v122, v123\n\t"
    "v_cvt_pk_bf16_f32 v118, v124, v125\n\t"
    "v_cvt_pk_bf16_f32 v119, v126, v127\n\t"
    "v_permlane32_swap_b32 v112, v114\n\t"
    "v_permlane32_swap_b32 v113, v115\n\t"
    "v_permlane32_swap_b32 v116, v118\n\t"
    "v_permlane32_swap_b32 v117, v119\n\t"
    "buffer_store_dwordx4 v[112:115], v241, %3, s45 offen offset:192\n\t"
    "buffer_store_dwordx4 v[116:119], v241, %3, s45 offen offset:224\n\t"
    "s_waitcnt vmcnt(0)\n\t"
    "s_branch END%=\n\t"
    "RARE1_1%=:\n\t"
    "s_nop 7\n\t"
    "s_nop 7\n\t"
    "v_mov_b32 v252, v248\n\t"
    "s_nop 1\n\t"
    "v_permlane32_swap_b32 v248, v252\n\t"
    "v_max_f32 v248, v248, v252\n\t"
    "v_max_f32 v253, 0, v248\n\t"
    "v_add_f32 v242, v242, v253\n\t"
    "v_sub_f32 v250, 0, v253\n\t"
    "v_exp_f32 v250, v250\n\t"
    "s_nop 0\n\t"
    "v_mul_f32 v244, v244, v250\n\t"
    "v_mul_f32 v245, v245, v250\n\t"
    "v_sub_f32 v64, v64, v253\n\t"
    "v_sub_f32 v65, v65, v253\n\t"
    "v_sub_f32 v66, v66, v253\n\t"
    "v_sub_f32 v67, v67, v253\n\t"
    "v_sub_f32 v68, v68, v253\n\t"
    "v_sub_f32 v69, v69, v253\n\t"
    "v_sub_f32 v70, v70, v253\n\t"
    "v_sub_f32 v71, v71, v253\n\t"
    "v_sub_f32 v72, v72, v253\n\t"
    "v_sub_f32 v73, v73, v253\n\t"
    "v_sub_f32 v74, v74, v253\n\t"
    "v_sub_f32 v75, v75, v253\n\t"
    "v_sub_f32 v76, v76, v253\n\t"
    "v_sub_f32 v77, v77, v253\n\t"
    "v_sub_f32 v78, v78, v253\n\t"
    "v_sub_f32 v79, v79, v253\n\t"
    "v_sub_f32 v96, v96, v253\n\t"
    "v_sub_f32 v97, v97, v253\n\t"
    "v_sub_f32 v98, v98, v253\n\t"
    "v_sub_f32 v99, v99, v253\n\t"
    "v_sub_f32 v100, v100, v253\n\t"
    "v_sub_f32 v101, v101, v253\n\t"
    "v_sub_f32 v102, v102, v253\n\t"
    "v_sub_f32 v103, v103, v253\n\t"
    "v_sub_f32 v104, v104, v253\n\t"
    "v_sub_f32 v105, v105, v253\n\t"
    "v_sub_f32 v106, v106, v253\n\t"
    "v_sub_f32 v107, v107, v253\n\t"
    "v_sub_f32 v108, v108, v253\n\t"
    "v_sub_f32 v109, v109, v253\n\t"
    "v_sub_f32 v110, v110, v253\n\t"
    "v_sub_f32 v111, v111, v253\n\t"
    "v_sub_f32 v252, 0, v242\n\t"
    "v_mov_b32 v192, v252\n\t"
    "v_mov_b32 v193, v252\n\t"
    "v_mov_b32 v194, v252\n\t"
    "v_mov_b32 v195, v252\n\t"
    "v_mov_b32 v196, v252\n\t"
    "v_mov_b32 v197, v252\n\t"
    "v_mov_b32 v198, v252\n\t"
    "v_mov_b32 v199, v252\n\t"
    "v_mov_b32 v200, v252\n\t"
    "v_mov_b32 v201, v252\n\t"
    "v_mov_b32 v202, v252\n\t"
    "v_mov_b32 v203, v252\n\t"
    "v_mov_b32 v204, v252\n\t"
    "v_mov_b32 v205, v252\n\t"
    "v_mov_b32 v206, v252\n\t"
    "v_mov_b32 v207, v252\n\t"
    "v_mov_b32 v252, v249\n\t"
    "s_nop 1\n\t"
    "v_permlane32_swap_b32 v249, v252\n\t"
    "v_max_f32 v249, v249, v252\n\t"
    "v_max_f32 v253, 0, v249\n\t"
    "v_add_f32 v243, v243, v253\n\t"
    "v_sub_f32 v251, 0, v253\n\t"
    "v_exp_f32 v251, v251\n\t"
    "s_nop 0\n\t"
    "v_mul_f32 v246, v246, v251\n\t"
    "v_mul_f32 v247, v247, v251\n\t"
    "v_sub_f32 v80, v80, v253\n\t"
    "v_sub_f32 v81, v81, v253\n\t"
    "v_sub_f32 v82, v82, v253\n\t"
    "v_sub_f32 v83, v83, v253\n\t"
    "v_sub_f32 v84, v84, v253\n\t"
    "v_sub_f32 v85, v85, v253\n\t"
    "v_sub_f32 v86, v86, v253\n\t"
    "v_sub_f32 v87, v87, v253\n\t"
    "v_sub_f32 v88, v88, v253\n\t"
    "v_sub_f32 v89, v89, v253\n\t"
    "v_sub_f32 v90, v90, v253\n\t"
    "v_sub_f32 v91, v91, v253\n\t"
    "v_sub_f32 v92, v92, v253\n\t"
    "v_sub_f32 v93, v93, v253\n\t"
    "v_sub_f32 v94, v94, v253\n\t"
    "v_sub_f32 v95, v95, v253\n\t"
    "v_sub_f32 v112, v112, v253\n\t"
    "v_sub_f32 v113, v113, v253\n\t"
    "v_sub_f32 v114, v114, v253\n\t"
    "v_sub_f32 v115, v115, v253\n\t"
    "v_sub_f32 v116, v116, v253\n\t"
    "v_sub_f32 v117, v117, v253\n\t"
    "v_sub_f32 v118, v118, v253\n\t"
    "v_sub_f32 v119, v119, v253\n\t"
    "v_sub_f32 v120, v120, v253\n\t"
    "v_sub_f32 v121, v121, v253\n\t"
    "v_sub_f32 v122, v122, v253\n\t"
    "v_sub_f32 v123, v123, v253\n\t"
    "v_sub_f32 v124, v124, v253\n\t"
    "v_sub_f32 v125, v125, v253\n\t"
    "v_sub_f32 v126, v126, v253\n\t"
    "v_sub_f32 v127, v127, v253\n\t"
    "v_sub_f32 v252, 0, v243\n\t"
    "v_mov_b32 v208, v252\n\t"
    "v_mov_b32 v209, v252\n\t"
    "v_mov_b32 v210, v252\n\t"
    "v_mov_b32 v211, v252\n\t"
    "v_mov_b32 v212, v252\n\t"
    "v_mov_b32 v213, v252\n\t"
    "v_mov_b32 v214, v252\n\t"
    "v_mov_b32 v215, v252\n\t"
    "v_mov_b32 v216, v252\n\t"
    "v_mov_b32 v217, v252\n\t"
    "v_mov_b32 v218, v252\n\t"
    "v_mov_b32 v219, v252\n\t"
    "v_mov_b32 v220, v252\n\t"
    "v_mov_b32 v221, v252\n\t"
    "v_mov_b32 v222, v252\n\t"
    "v_mov_b32 v223, v252\n\t"
    "s_mov_b32 s46, 1\n\t"
    "s_nop 7\n\t"
    "s_branch RARE1R_1%=\n\t"
    "RARE2_1%=:\n\t"
    "s_nop 7\n\t"
    "s_nop 7\n\t"
    "v_accvgpr_read_b32 v252, a0\n\t"
    "v_mul_f32 v252, v252, v250\n\t"
    "v_accvgpr_write_b32 a0, v252\n\t"
    "v_accvgpr_read_b32 v253, a1\n\t"
    "v_mul_f32 v253, v253, v250\n\t"
    "v_accvgpr_write_b32 a1, v253\n\t"
    "v_accvgpr_read_b32 v252, a2\n\t"
    "v_mul_f32 v252, v252, v250\n\t"
    "v_accvgpr_write_b32 a2, v252\n\t"
    "v_accvgpr_read_b32 v253, a3\n\t"
    "v_mul_f32 v253, v253, v250\n\t"
    "v_accvgpr_write_b32 a3, v253\n\t"
    "v_accvgpr_read_b32 v252, a4\n\t"
    "v_mul_f32 v252, v252, v250\n\t"
    "v_accvgpr_write_b32 a4, v252\n\t"
    "v_accvgpr_read_b32 v253, a5\n\t"
    "v_mul_f32 v253, v253, v250\n\t"
    "v_accvgpr_write_b32 a5, v253\n\t"
    "v_accvgpr_read_b32 v252, a6\n\t"
    "v_mul_f32 v252, v252, v250\n\t"
    "v_accvgpr_write_b32 a6, v252\n\t"
    "v_accvgpr_read_b32 v253, a7\n\t"
    "v_mul_f32 v253, v253, v250\n\t"
    "v_accvgpr_write_b32 a7, v253\n\t"
    "v_accvgpr_read_b32 v252, a8\n\t"
    "v_mul_f32 v252, v252, v250\n\t"
    "v_accvgpr_write_b32 a8, v252\n\t"
    "v_accvgpr_read_b32 v253, a9\n\t"
    "v_mul_f32 v253, v253, v250\n\t"
    "v_accvgpr_write_b32 a9, v253\n\t"
    "v_accvgpr_read_b32 v252, a10\n\t"
    "v_mul_f32 v252, v252, v250\n\t"
    "v_accvgpr_write_b32 a10, v252\n\t"
    "v_accvgpr_read_b32 v253, a11\n\t"
    "v_mul_f32 v253, v253, v250\n\t"
    "v_accvgpr_write_b32 a11, v253\n\t"
    "v_accvgpr_read_b32 v252, a12\n\t"
    "v_mul_f32 v252, v252, v250\n\t"
    "v_accvgpr_write_b32 a12, v252\n\t"
    "v_accvgpr_read_b32 v253, a13\n\t"
    "v_mul_f32 v253, v253, v250\n\t"
    "v_accvgpr_write_b32 a13, v253\n\t"
    "v_accvgpr_read_b32 v252, a14\n\t"
    "v_mul_f32 v252, v252, v250\n\t"
    "v_accvgpr_write_b32 a14, v252\n\t"
    "v_accvgpr_read_b32 v253, a15\n\t"
    "v_mul_f32 v253, v253, v250\n\t"
    "v_accvgpr_write_b32 a15, v253\n\t"
    "v_accvgpr_read_b32 v252, a16\n\t"
    "v_mul_f32 v252, v252, v251\n\t"
    "v_accvgpr_write_b32 a16, v252\n\t"
    "v_accvgpr_read_b32 v253, a17\n\t"
    "v_mul_f32 v253, v253, v251\n\t"
    "v_accvgpr_write_b32 a17, v253\n\t"
    "v_accvgpr_read_b32 v252, a18\n\t"
    "v_mul_f32 v252, v252, v251\n\t"
    "v_accvgpr_write_b32 a18, v252\n\t"
    "v_accvgpr_read_b32 v253, a19\n\t"
    "v_mul_f32 v253, v253, v251\n\t"
    "v_accvgpr_write_b32 a19, v253\n\t"
    "v_accvgpr_read_b32 v252, a20\n\t"
    "v_mul_f32 v252, v252, v251\n\t"
    "v_accvgpr_write_b32 a20, v252\n\t"
    "v_accvgpr_read_b32 v253, a21\n\t"
    "v_mul_f32 v253, v253, v251\n\t"
    "v_accvgpr_write_b32 a21, v253\n\t"
    "v_accvgpr_read_b32 v252, a22\n\t"
    "v_mul_f32 v252, v252, v251\n\t"
    "v_accvgpr_write_b32 a22, v252\n\t"
    "v_accvgpr_read_b32 v253, a23\n\t"
    "v_mul_f32 v253, v253, v251\n\t"
    "v_accvgpr_write_b32 a23, v253\n\t"
    "v_accvgpr_read_b32 v252, a24\n\t"
    "v_mul_f32 v252, v252, v251\n\t"
    "v_accvgpr_write_b32 a24, v252\n\t"
    "v_accvgpr_read_b32 v253, a25\n\t"
    "v_mul_f32 v253, v253, v251\n\t"
    "v_accvgpr_write_b32 a25, v253\n\t"
    "v_accvgpr_read_b32 v252, a26\n\t"
    "v_mul_f32 v252, v252, v251\n\t"
    "v_accvgpr_write_b32 a26, v252\n\t"
    "v_accvgpr_read_b32 v253, a27\n\t"
    "v_mul_f32 v253, v253, v251\n\t"
    "v_accvgpr_write_b32 a27, v253\n\t"
    "v_accvgpr_read_b32 v252, a28\n\t"
    "v_mul_f32 v252, v252, v251\n\t"
    "v_accvgpr_write_b32 a28, v252\n\t"
    "v_accvgpr_read_b32 v253, a29\n\t"
    "v_mul_f32 v253, v253, v251\n\t"
    "v_accvgpr_write_b32 a29, v253\n\t"
    "v_accvgpr_read_b32 v252, a30\n\t"
    "v_mul_f32 v252, v252, v251\n\t"
    "v_accvgpr_write_b32 a30, v252\n\t"
    "v_accvgpr_read_b32 v253, a31\n\t"
    "v_mul_f32 v253, v253, v251\n\t"
    "v_accvgpr_write_b32 a31, v253\n\t"
    "v_accvgpr_read_b32 v252, a32\n\t"
    "v_mul_f32 v252, v252, v250\n\t"
    "v_accvgpr_write_b32 a32, v252\n\t"
    "v_accvgpr_read_b32 v253, a33\n\t"
    "v_mul_f32 v253, v253, v250\n\t"
    "v_accvgpr_write_b32 a33, v253\n\t"
    "v_accvgpr_read_b32 v252, a34\n\t"
    "v_mul_f32 v252, v252, v250\n\t"
    "v_accvgpr_write_b32 a34, v252\n\t"
    "v_accvgpr_read_b32 v253, a35\n\t"
    "v_mul_f32 v253, v253, v250\n\t"
    "v_accvgpr_write_b32 a35, v253\n\t"
    "v_accvgpr_read_b32 v252, a36\n\t"
    "v_mul_f32 v252, v252, v250\n\t"
    "v_accvgpr_write_b32 a36, v252\n\t"
    "v_accvgpr_read_b32 v253, a37\n\t"
    "v_mul_f32 v253, v253, v250\n\t"
    "v_accvgpr_write_b32 a37, v253\n\t"
    "v_accvgpr_read_b32 v252, a38\n\t"
    "v_mul_f32 v252, v252, v250\n\t"
    "v_accvgpr_write_b32 a38, v252\n\t"
    "v_accvgpr_read_b32 v253, a39\n\t"
    "v_mul_f32 v253, v253, v250\n\t"
    "v_accvgpr_write_b32 a39, v253\n\t"
    "v_accvgpr_read_b32 v252, a40\n\t"
    "v_mul_f32 v252, v252, v250\n\t"
    "v_accvgpr_write_b32 a40, v252\n\t"
    "v_accvgpr_read_b32 v253, a41\n\t"
    "v_mul_f32 v253, v253, v250\n\t"
    "v_accvgpr_write_b32 a41, v253\n\t"
    "v_accvgpr_read_b32 v252, a42\n\t"
    "v_mul_f32 v252, v252, v250\n\t"
    "v_accvgpr_write_b32 a42, v252\n\t"
    "v_accvgpr_read_b32 v253, a43\n\t"
    "v_mul_f32 v253, v253, v250\n\t"
    "v_accvgpr_write_b32 a43, v253\n\t"
    "v_accvgpr_read_b32 v252, a44\n\t"
    "v_mul_f32 v252, v252, v250\n\t"
    "v_accvgpr_write_b32 a44, v252\n\t"
    "v_accvgpr_read_b32 v253, a45\n\t"
    "v_mul_f32 v253, v253, v250\n\t"
    "v_accvgpr_write_b32 a45, v253\n\t"
    "v_accvgpr_read_b32 v252, a46\n\t"
    "v_mul_f32 v252, v252, v250\n\t"
    "v_accvgpr_write_b32 a46, v252\n\t"
    "v_accvgpr_read_b32 v253, a47\n\t"
    "v_mul_f32 v253, v253, v250\n\t"
    "v_accvgpr_write_b32 a47, v253\n\t"
    "v_accvgpr_read_b32 v252, a48\n\t"
    "v_mul_f32 v252, v252, v251\n\t"
    "v_accvgpr_write_b32 a48, v252\n\t"
    "v_accvgpr_read_b32 v253, a49\n\t"
    "v_mul_f32 v253, v253, v251\n\t"
    "v_accvgpr_write_b32 a49, v253\n\t"
    "v_accvgpr_read_b32 v252, a50\n\t"
    "v_mul_f32 v252, v252, v251\n\t"
    "v_accvgpr_write_b32 a50, v252\n\t"
    "v_accvgpr_read_b32 v253, a51\n\t"
    "v_mul_f32 v253, v253, v251\n\t"
    "v_accvgpr_write_b32 a51, v253\n\t"
    "v_accvgpr_read_b32 v252, a52\n\t"
    "v_mul_f32 v252, v252, v251\n\t"
    "v_accvgpr_write_b32 a52, v252\n\t"
    "v_accvgpr_read_b32 v253, a53\n\t"
    "v_mul_f32 v253, v253, v251\n\t"
    "v_accvgpr_write_b32 a53, v253\n\t"
    "v_accvgpr_read_b32 v252, a54\n\t"
    "v_mul_f32 v252, v252, v251\n\t"
    "v_accvgpr_write_b32 a54, v252\n\t"
    "v_accvgpr_read_b32 v253, a55\n\t"
    "v_mul_f32 v253, v253, v251\n\t"
    "v_accvgpr_write_b32 a55, v253\n\t"
    "v_accvgpr_read_b32 v252, a56\n\t"
    "v_mul_f32 v252, v252, v251\n\t"
    "v_accvgpr_write_b32 a56, v252\n\t"
    "v_accvgpr_read_b32 v253, a57\n\t"
    "v_mul_f32 v253, v253, v251\n\t"
    "v_accvgpr_write_b32 a57, v253\n\t"
    "v_accvgpr_read_b32 v252, a58\n\t"
    "v_mul_f32 v252, v252, v251\n\t"
    "v_accvgpr_write_b32 a58, v252\n\t"
    "v_accvgpr_read_b32 v253, a59\n\t"
    "v_mul_f32 v253, v253, v251\n\t"
    "v_accvgpr_write_b32 a59, v253\n\t"
    "v_accvgpr_read_b32 v252, a60\n\t"
    "v_mul_f32 v252, v252, v251\n\t"
    "v_accvgpr_write_b32 a60, v252\n\t"
    "v_accvgpr_read_b32 v253, a61\n\t"
    "v_mul_f32 v253, v253, v251\n\t"
    "v_accvgpr_write_b32 a61, v253\n\t"
    "v_accvgpr_read_b32 v252, a62\n\t"
    "v_mul_f32 v252, v252, v251\n\t"
    "v_accvgpr_write_b32 a62, v252\n\t"
    "v_accvgpr_read_b32 v253, a63\n\t"
    "v_mul_f32 v253, v253, v251\n\t"
    "v_accvgpr_write_b32 a63, v253\n\t"
    "v_accvgpr_read_b32 v252, a64\n\t"
    "v_mul_f32 v252, v252, v250\n\t"
    "v_accvgpr_write_b32 a64, v252\n\t"
    "v_accvgpr_read_b32 v253, a65\n\t"
    "v_mul_f32 v253, v253, v250\n\t"
    "v_accvgpr_write_b32 a65, v253\n\t"
    "v_accvgpr_read_b32 v252, a66\n\t"
    "v_mul_f32 v252, v252, v250\n\t"
    "v_accvgpr_write_b32 a66, v252\n\t"
    "v_accvgpr_read_b32 v253, a67\n\t"
    "v_mul_f32 v253, v253, v250\n\t"
    "v_accvgpr_write_b32 a67, v253\n\t"
    "v_accvgpr_read_b32 v252, a68\n\t"
    "v_mul_f32 v252, v252, v250\n\t"
    "v_accvgpr_write_b32 a68, v252\n\t"
    "v_accvgpr_read_b32 v253, a69\n\t"
    "v_mul_f32 v253, v253, v250\n\t"
    "v_accvgpr_write_b32 a69, v253\n\t"
    "v_accvgpr_read_b32 v252, a70\n\t"
    "v_mul_f32 v252, v252, v250\n\t"
    "v_accvgpr_write_b32 a70, v252\n\t"
    "v_accvgpr_read_b32 v253, a71\n\t"
    "v_mul_f32 v253, v253, v250\n\t"
    "v_accvgpr_write_b32 a71, v253\n\t"
    "v_accvgpr_read_b32 v252, a72\n\t"
    "v_mul_f32 v252, v252, v250\n\t"
    "v_accvgpr_write_b32 a72, v252\n\t"
    "v_accvgpr_read_b32 v253, a73\n\t"
    "v_mul_f32 v253, v253, v250\n\t"
    "v_accvgpr_write_b32 a73, v253\n\t"
    "v_accvgpr_read_b32 v252, a74\n\t"
    "v_mul_f32 v252, v252, v250\n\t"
    "v_accvgpr_write_b32 a74, v252\n\t"
    "v_accvgpr_read_b32 v253, a75\n\t"
    "v_mul_f32 v253, v253, v250\n\t"
    "v_accvgpr_write_b32 a75, v253\n\t"
    "v_accvgpr_read_b32 v252, a76\n\t"
    "v_mul_f32 v252, v252, v250\n\t"
    "v_accvgpr_write_b32 a76, v252\n\t"
    "v_accvgpr_read_b32 v253, a77\n\t"
    "v_mul_f32 v253, v253, v250\n\t"
    "v_accvgpr_write_b32 a77, v253\n\t"
    "v_accvgpr_read_b32 v252, a78\n\t"
    "v_mul_f32 v252, v252, v250\n\t"
    "v_accvgpr_write_b32 a78, v252\n\t"
    "v_accvgpr_read_b32 v253, a79\n\t"
    "v_mul_f32 v253, v253, v250\n\t"
    "v_accvgpr_write_b32 a79, v253\n\t"
    "v_accvgpr_read_b32 v252, a80\n\t"
    "v_mul_f32 v252, v252, v251\n\t"
    "v_accvgpr_write_b32 a80, v252\n\t"
    "v_accvgpr_read_b32 v253, a81\n\t"
    "v_mul_f32 v253, v253, v251\n\t"
    "v_accvgpr_write_b32 a81, v253\n\t"
    "v_accvgpr_read_b32 v252, a82\n\t"
    "v_mul_f32 v252, v252, v251\n\t"
    "v_accvgpr_write_b32 a82, v252\n\t"
    "v_accvgpr_read_b32 v253, a83\n\t"
    "v_mul_f32 v253, v253, v251\n\t"
    "v_accvgpr_write_b32 a83, v253\n\t"
    "v_accvgpr_read_b32 v252, a84\n\t"
    "v_mul_f32 v252, v252, v251\n\t"
    "v_accvgpr_write_b32 a84, v252\n\t"
    "v_accvgpr_read_b32 v253, a85\n\t"
    "v_mul_f32 v253, v253, v251\n\t"
    "v_accvgpr_write_b32 a85, v253\n\t"
    "v_accvgpr_read_b32 v252, a86\n\t"
    "v_mul_f32 v252, v252, v251\n\t"
    "v_accvgpr_write_b32 a86, v252\n\t"
    "v_accvgpr_read_b32 v253, a87\n\t"
    "v_mul_f32 v253, v253, v251\n\t"
    "v_accvgpr_write_b32 a87, v253\n\t"
    "v_accvgpr_read_b32 v252, a88\n\t"
    "v_mul_f32 v252, v252, v251\n\t"
    "v_accvgpr_write_b32 a88, v252\n\t"
    "v_accvgpr_read_b32 v253, a89\n\t"
    "v_mul_f32 v253, v253, v251\n\t"
    "v_accvgpr_write_b32 a89, v253\n\t"
    "v_accvgpr_read_b32 v252, a90\n\t"
    "v_mul_f32 v252, v252, v251\n\t"
    "v_accvgpr_write_b32 a90, v252\n\t"
    "v_accvgpr_read_b32 v253, a91\n\t"
    "v_mul_f32 v253, v253, v251\n\t"
    "v_accvgpr_write_b32 a91, v253\n\t"
    "v_accvgpr_read_b32 v252, a92\n\t"
    "v_mul_f32 v252, v252, v251\n\t"
    "v_accvgpr_write_b32 a92, v252\n\t"
    "v_accvgpr_read_b32 v253, a93\n\t"
    "v_mul_f32 v253, v253, v251\n\t"
    "v_accvgpr_write_b32 a93, v253\n\t"
    "v_accvgpr_read_b32 v252, a94\n\t"
    "v_mul_f32 v252, v252, v251\n\t"
    "v_accvgpr_write_b32 a94, v252\n\t"
    "v_accvgpr_read_b32 v253, a95\n\t"
    "v_mul_f32 v253, v253, v251\n\t"
    "v_accvgpr_write_b32 a95, v253\n\t"
    "v_accvgpr_read_b32 v252, a96\n\t"
    "v_mul_f32 v252, v252, v250\n\t"
    "v_accvgpr_write_b32 a96, v252\n\t"
    "v_accvgpr_read_b32 v253, a97\n\t"
    "v_mul_f32 v253, v253, v250\n\t"
    "v_accvgpr_write_b32 a97, v253\n\t"
    "v_accvgpr_read_b32 v252, a98\n\t"
    "v_mul_f32 v252, v252, v250\n\t"
    "v_accvgpr_write_b32 a98, v252\n\t"
    "v_accvgpr_read_b32 v253, a99\n\t"
    "v_mul_f32 v253, v253, v250\n\t"
    "v_accvgpr_write_b32 a99, v253\n\t"
    "v_accvgpr_read_b32 v252, a100\n\t"
    "v_mul_f32 v252, v252, v250\n\t"
    "v_accvgpr_write_b32 a100, v252\n\t"
    "v_accvgpr_read_b32 v253, a101\n\t"
    "v_mul_f32 v253, v253, v250\n\t"
    "v_accvgpr_write_b32 a101, v253\n\t"
    "v_accvgpr_read_b32 v252, a102\n\t"
    "v_mul_f32 v252, v252, v250\n\t"
    "v_accvgpr_write_b32 a102, v252\n\t"
    "v_accvgpr_read_b32 v253, a103\n\t"
    "v_mul_f32 v253, v253, v250\n\t"
    "v_accvgpr_write_b32 a103, v253\n\t"
    "v_accvgpr_read_b32 v252, a104\n\t"
    "v_mul_f32 v252, v252, v250\n\t"
    "v_accvgpr_write_b32 a104, v252\n\t"
    "v_accvgpr_read_b32 v253, a105\n\t"
    "v_mul_f32 v253, v253, v250\n\t"
    "v_accvgpr_write_b32 a105, v253\n\t"
    "v_accvgpr_read_b32 v252, a106\n\t"
    "v_mul_f32 v252, v252, v250\n\t"
    "v_accvgpr_write_b32 a106, v252\n\t"
    "v_accvgpr_read_b32 v253, a107\n\t"
    "v_mul_f32 v253, v253, v250\n\t"
    "v_accvgpr_write_b32 a107, v253\n\t"
    "v_accvgpr_read_b32 v252, a108\n\t"
    "v_mul_f32 v252, v252, v250\n\t"
    "v_accvgpr_write_b32 a108, v252\n\t"
    "v_accvgpr_read_b32 v253, a109\n\t"
    "v_mul_f32 v253, v253, v250\n\t"
    "v_accvgpr_write_b32 a109, v253\n\t"
    "v_accvgpr_read_b32 v252, a110\n\t"
    "v_mul_f32 v252, v252, v250\n\t"
    "v_accvgpr_write_b32 a110, v252\n\t"
    "v_accvgpr_read_b32 v253, a111\n\t"
    "v_mul_f32 v253, v253, v250\n\t"
    "v_accvgpr_write_b32 a111, v253\n\t"
    "v_accvgpr_read_b32 v252, a112\n\t"
    "v_mul_f32 v252, v252, v251\n\t"
    "v_accvgpr_write_b32 a112, v252\n\t"
    "v_accvgpr_read_b32 v253, a113\n\t"
    "v_mul_f32 v253, v253, v251\n\t"
    "v_accvgpr_write_b32 a113, v253\n\t"
    "v_accvgpr_read_b32 v252, a114\n\t"
    "v_mul_f32 v252, v252, v251\n\t"
    "v_accvgpr_write_b32 a114, v252\n\t"
    "v_accvgpr_read_b32 v253, a115\n\t"
    "v_mul_f32 v253, v253, v251\n\t"
    "v_accvgpr_write_b32 a115, v253\n\t"
    "v_accvgpr_read_b32 v252, a116\n\t"
    "v_mul_f32 v252, v252, v251\n\t"
    "v_accvgpr_write_b32 a116, v252\n\t"
    "v_accvgpr_read_b32 v253, a117\n\t"
    "v_mul_f32 v253, v253, v251\n\t"
    "v_accvgpr_write_b32 a117, v253\n\t"
    "v_accvgpr_read_b32 v252, a118\n\t"
    "v_mul_f32 v252, v252, v251\n\t"
    "v_accvgpr_write_b32 a118, v252\n\t"
    "v_accvgpr_read_b32 v253, a119\n\t"
    "v_mul_f32 v253, v253, v251\n\t"
    "v_accvgpr_write_b32 a119, v253\n\t"
    "v_accvgpr_read_b32 v252, a120\n\t"
    "v_mul_f32 v252, v252, v251\n\t"
    "v_accvgpr_write_b32 a120, v252\n\t"
    "v_accvgpr_read_b32 v253, a121\n\t"
    "v_mul_f32 v253, v253, v251\n\t"
    "v_accvgpr_write_b32 a121, v253\n\t"
    "v_accvgpr_read_b32 v252, a122\n\t"
    "v_mul_f32 v252, v252, v251\n\t"
    "v_accvgpr_write_b32 a122, v252\n\t"
    "v_accvgpr_read_b32 v253, a123\n\t"
    "v_mul_f32 v253, v253, v251\n\t"
    "v_accvgpr_write_b32 a123, v253\n\t"
    "v_accvgpr_read_b32 v252, a124\n\t"
    "v_mul_f32 v252, v252, v251\n\t"
    "v_accvgpr_write_b32 a124, v252\n\t"
    "v_accvgpr_read_b32 v253, a125\n\t"
    "v_mul_f32 v253, v253, v251\n\t"
    "v_accvgpr_write_b32 a125, v253\n\t"
    "v_accvgpr_read_b32 v252, a126\n\t"
    "v_mul_f32 v252, v252, v251\n\t"
    "v_accvgpr_write_b32 a126, v252\n\t"
    "v_accvgpr_read_b32 v253, a127\n\t"
    "v_mul_f32 v253, v253, v251\n\t"
    "v_accvgpr_write_b32 a127, v253\n\t"
    "s_mov_b32 s46, 0\n\t"
    "s_nop 7\n\t"
    "s_branch RARE2R_1%=\n\t"
    "RARE1_2%=:\n\t"
    "s_nop 7\n\t"
    "s_nop 7\n\t"
    "v_mov_b32 v252, v248\n\t"
    "s_nop 1\n\t"
    "v_permlane32_swap_b32 v248, v252\n\t"
    "v_max_f32 v248, v248, v252\n\t"
    "v_max_f32 v253, 0, v248\n\t"
    "v_add_f32 v242, v242, v253\n\t"
    "v_sub_f32 v250, 0, v253\n\t"
    "v_exp_f32 v250, v250\n\t"
    "s_nop 0\n\t"
    "v_mul_f32 v244, v244, v250\n\t"
    "v_mul_f32 v245, v245, v250\n\t"
    "v_sub_f32 v0, v0, v253\n\t"
    "v_sub_f32 v1, v1, v253\n\t"
    "v_sub_f32 v2, v2, v253\n\t"
    "v_sub_f32 v3, v3, v253\n\t"
    "v_sub_f32 v4, v4, v253\n\t"
    "v_sub_f32 v5, v5, v253\n\t"
    "v_sub_f32 v6, v6, v253\n\t"
    "v_sub_f32 v7, v7, v253\n\t"
    "v_sub_f32 v8, v8, v253\n\t"
    "v_sub_f32 v9, v9, v253\n\t"
    "v_sub_f32 v10, v10, v253\n\t"
    "v_sub_f32 v11, v11, v253\n\t"
    "v_sub_f32 v12, v12, v253\n\t"
    "v_sub_f32 v13, v13, v253\n\t"
    "v_sub_f32 v14, v14, v253\n\t"
    "v_sub_f32 v15, v15, v253\n\t"
    "v_sub_f32 v32, v32, v253\n\t"
    "v_sub_f32 v33, v33, v253\n\t"
    "v_sub_f32 v34, v34, v253\n\t"
    "v_sub_f32 v35, v35, v253\n\t"
    "v_sub_f32 v36, v36, v253\n\t"
    "v_sub_f32 v37, v37, v253\n\t"
    "v_sub_f32 v38, v38, v253\n\t"
    "v_sub_f32 v39, v39, v253\n\t"
    "v_sub_f32 v40, v40, v253\n\t"
    "v_sub_f32 v41, v41, v253\n\t"
    "v_sub_f32 v42, v42, v253\n\t"
    "v_sub_f32 v43, v43, v253\n\t"
    "v_sub_f32 v44, v44, v253\n\t"
    "v_sub_f32 v45, v45, v253\n\t"
    "v_sub_f32 v46, v46, v253\n\t"
    "v_sub_f32 v47, v47, v253\n\t"
    "v_sub_f32 v252, 0, v242\n\t"
    "v_mov_b32 v192, v252\n\t"
    "v_mov_b32 v193, v252\n\t"
    "v_mov_b32 v194, v252\n\t"
    "v_mov_b32 v195, v252\n\t"
    "v_mov_b32 v196, v252\n\t"
    "v_mov_b32 v197, v252\n\t"
    "v_mov_b32 v198, v252\n\t"
    "v_mov_b32 v199, v252\n\t"
    "v_mov_b32 v200, v252\n\t"
    "v_mov_b32 v201, v252\n\t"
    "v_mov_b32 v202, v252\n\t"
    "v_mov_b32 v203, v252\n\t"
    "v_mov_b32 v204, v252\n\t"
    "v_mov_b32 v205, v252\n\t"
    "v_mov_b32 v206, v252\n\t"
    "v_mov_b32 v207, v252\n\t"
    "v_mov_b32 v252, v249\n\t"
    "s_nop 1\n\t"
    "v_permlane32_swap_b32 v249, v252\n\t"
    "v_max_f32 v249, v249, v252\n\t"
    "v_max_f32 v253, 0, v249\n\t"
    "v_add_f32 v243, v243, v253\n\t"
    "v_sub_f32 v251, 0, v253\n\t"
    "v_exp_f32 v251, v251\n\t"
    "s_nop 0\n\t"
    "v_mul_f32 v246, v246, v251\n\t"
    "v_mul_f32 v247, v247, v251\n\t"
    "v_sub_f32 v16, v16, v253\n\t"
    "v_sub_f32 v17, v17, v253\n\t"
    "v_sub_f32 v18, v18, v253\n\t"
    "v_sub_f32 v19, v19, v253\n\t"
    "v_sub_f32 v20, v20, v253\n\t"
    "v_sub_f32 v21, v21, v253\n\t"
    "v_sub_f32 v22, v22, v253\n\t"
    "v_sub_f32 v23, v23, v253\n\t"
    "v_sub_f32 v24, v24, v253\n\t"
    "v_sub_f32 v25, v25, v253\n\t"
    "v_sub_f32 v26, v26, v253\n\t"
    "v_sub_f32 v27, v27, v253\n\t"
    "v_sub_f32 v28, v28, v253\n\t"
    "v_sub_f32 v29, v29, v253\n\t"
    "v_sub_f32 v30, v30, v253\n\t"
    "v_sub_f32 v31, v31, v253\n\t"
    "v_sub_f32 v48, v48, v253\n\t"
    "v_sub_f32 v49, v49, v253\n\t"
    "v_sub_f32 v50, v50, v253\n\t"
    "v_sub_f32 v51, v51, v253\n\t"
    "v_sub_f32 v52, v52, v253\n\t"
    "v_sub_f32 v53, v53, v253\n\t"
    "v_sub_f32 v54, v54, v253\n\t"
    "v_sub_f32 v55, v55, v253\n\t"
    "v_sub_f32 v56, v56, v253\n\t"
    "v_sub_f32 v57, v57, v253\n\t"
    "v_sub_f32 v58, v58, v253\n\t"
    "v_sub_f32 v59, v59, v253\n\t"
    "v_sub_f32 v60, v60, v253\n\t"
    "v_sub_f32 v61, v61, v253\n\t"
    "v_sub_f32 v62, v62, v253\n\t"
    "v_sub_f32 v63, v63, v253\n\t"
    "v_sub_f32 v252, 0, v243\n\t"
    "v_mov_b32 v208, v252\n\t"
    "v_mov_b32 v209, v252\n\t"
    "v_mov_b32 v210, v252\n\t"
    "v_mov_b32 v211, v252\n\t"
    "v_mov_b32 v212, v252\n\t"
    "v_mov_b32 v213, v252\n\t"
    "v_mov_b32 v214, v252\n\t"
    "v_mov_b32 v215, v252\n\t"
    "v_mov_b32 v216, v252\n\t"
    "v_mov_b32 v217, v252\n\t"
    "v_mov_b32 v218, v252\n\t"
    "v_mov_b32 v219, v252\n\t"
    "v_mov_b32 v220, v252\n\t"
    "v_mov_b32 v221, v252\n\t"
    "v_mov_b32 v222, v252\n\t"
    "v_mov_b32 v223, v252\n\t"
    "s_mov_b32 s46, 1\n\t"
    "s_nop 7\n\t"
    "s_branch RARE1R_2%=\n\t"
    "RARE2_2%=:\n\t"
    "s_nop 7\n\t"
    "s_nop 7\n\t"
    "v_accvgpr_read_b32 v252, a0\n\t"
    "v_mul_f32 v252, v252, v250\n\t"
    "v_accvgpr_write_b32 a0, v252\n\t"
    "v_accvgpr_read_b32 v253, a1\n\t"
    "v_mul_f32 v253, v253, v250\n\t"
    "v_accvgpr_write_b32 a1, v253\n\t"
    "v_accvgpr_read_b32 v252, a2\n\t"
    "v_mul_f32 v252, v252, v250\n\t"
    "v_accvgpr_write_b32 a2, v252\n\t"
    "v_accvgpr_read_b32 v253, a3\n\t"
    "v_mul_f32 v253, v253, v250\n\t"
    "v_accvgpr_write_b32 a3, v253\n\t"
    "v_accvgpr_read_b32 v252, a4\n\t"
    "v_mul_f32 v252, v252, v250\n\t"
    "v_accvgpr_write_b32 a4, v252\n\t"
    "v_accvgpr_read_b32 v253, a5\n\t"
    "v_mul_f32 v253, v253, v250\n\t"
    "v_accvgpr_write_b32 a5, v253\n\t"
    "v_accvgpr_read_b32 v252, a6\n\t"
    "v_mul_f32 v252, v252, v250\n\t"
    "v_accvgpr_write_b32 a6, v252\n\t"
    "v_accvgpr_read_b32 v253, a7\n\t"
    "v_mul_f32 v253, v253, v250\n\t"
    "v_accvgpr_write_b32 a7, v253\n\t"
    "v_accvgpr_read_b32 v252, a8\n\t"
    "v_mul_f32 v252, v252, v250\n\t"
    "v_accvgpr_write_b32 a8, v252\n\t"
    "v_accvgpr_read_b32 v253, a9\n\t"
    "v_mul_f32 v253, v253, v250\n\t"
    "v_accvgpr_write_b32 a9, v253\n\t"
    "v_accvgpr_read_b32 v252, a10\n\t"
    "v_mul_f32 v252, v252, v250\n\t"
    "v_accvgpr_write_b32 a10, v252\n\t"
    "v_accvgpr_read_b32 v253, a11\n\t"
    "v_mul_f32 v253, v253, v250\n\t"
    "v_accvgpr_write_b32 a11, v253\n\t"
    "v_accvgpr_read_b32 v252, a12\n\t"
    "v_mul_f32 v252, v252, v250\n\t"
    "v_accvgpr_write_b32 a12, v252\n\t"
    "v_accvgpr_read_b32 v253, a13\n\t"
    "v_mul_f32 v253, v253, v250\n\t"
    "v_accvgpr_write_b32 a13, v253\n\t"
    "v_accvgpr_read_b32 v252, a14\n\t"
    "v_mul_f32 v252, v252, v250\n\t"
    "v_accvgpr_write_b32 a14, v252\n\t"
    "v_accvgpr_read_b32 v253, a15\n\t"
    "v_mul_f32 v253, v253, v250\n\t"
    "v_accvgpr_write_b32 a15, v253\n\t"
    "v_accvgpr_read_b32 v252, a16\n\t"
    "v_mul_f32 v252, v252, v251\n\t"
    "v_accvgpr_write_b32 a16, v252\n\t"
    "v_accvgpr_read_b32 v253, a17\n\t"
    "v_mul_f32 v253, v253, v251\n\t"
    "v_accvgpr_write_b32 a17, v253\n\t"
    "v_accvgpr_read_b32 v252, a18\n\t"
    "v_mul_f32 v252, v252, v251\n\t"
    "v_accvgpr_write_b32 a18, v252\n\t"
    "v_accvgpr_read_b32 v253, a19\n\t"
    "v_mul_f32 v253, v253, v251\n\t"
    "v_accvgpr_write_b32 a19, v253\n\t"
    "v_accvgpr_read_b32 v252, a20\n\t"
    "v_mul_f32 v252, v252, v251\n\t"
    "v_accvgpr_write_b32 a20, v252\n\t"
    "v_accvgpr_read_b32 v253, a21\n\t"
    "v_mul_f32 v253, v253, v251\n\t"
    "v_accvgpr_write_b32 a21, v253\n\t"
    "v_accvgpr_read_b32 v252, a22\n\t"
    "v_mul_f32 v252, v252, v251\n\t"
    "v_accvgpr_write_b32 a22, v252\n\t"
    "v_accvgpr_read_b32 v253, a23\n\t"
    "v_mul_f32 v253, v253, v251\n\t"
    "v_accvgpr_write_b32 a23, v253\n\t"
    "v_accvgpr_read_b32 v252, a24\n\t"
    "v_mul_f32 v252, v252, v251\n\t"
    "v_accvgpr_write_b32 a24, v252\n\t"
    "v_accvgpr_read_b32 v253, a25\n\t"
    "v_mul_f32 v253, v253, v251\n\t"
    "v_accvgpr_write_b32 a25, v253\n\t"
    "v_accvgpr_read_b32 v252, a26\n\t"
    "v_mul_f32 v252, v252, v251\n\t"
    "v_accvgpr_write_b32 a26, v252\n\t"
    "v_accvgpr_read_b32 v253, a27\n\t"
    "v_mul_f32 v253, v253, v251\n\t"
    "v_accvgpr_write_b32 a27, v253\n\t"
    "v_accvgpr_read_b32 v252, a28\n\t"
    "v_mul_f32 v252, v252, v251\n\t"
    "v_accvgpr_write_b32 a28, v252\n\t"
    "v_accvgpr_read_b32 v253, a29\n\t"
    "v_mul_f32 v253, v253, v251\n\t"
    "v_accvgpr_write_b32 a29, v253\n\t"
    "v_accvgpr_read_b32 v252, a30\n\t"
    "v_mul_f32 v252, v252, v251\n\t"
    "v_accvgpr_write_b32 a30, v252\n\t"
    "v_accvgpr_read_b32 v253, a31\n\t"
    "v_mul_f32 v253, v253, v251\n\t"
    "v_accvgpr_write_b32 a31, v253\n\t"
    "v_accvgpr_read_b32 v252, a32\n\t"
    "v_mul_f32 v252, v252, v250\n\t"
    "v_accvgpr_write_b32 a32, v252\n\t"
    "v_accvgpr_read_b32 v253, a33\n\t"
    "v_mul_f32 v253, v253, v250\n\t"
    "v_accvgpr_write_b32 a33, v253\n\t"
    "v_accvgpr_read_b32 v252, a34\n\t"
    "v_mul_f32 v252, v252, v250\n\t"
    "v_accvgpr_write_b32 a34, v252\n\t"
    "v_accvgpr_read_b32 v253, a35\n\t"
    "v_mul_f32 v253, v253, v250\n\t"
    "v_accvgpr_write_b32 a35, v253\n\t"
    "v_accvgpr_read_b32 v252, a36\n\t"
    "v_mul_f32 v252, v252, v250\n\t"
    "v_accvgpr_write_b32 a36, v252\n\t"
    "v_accvgpr_read_b32 v253, a37\n\t"
    "v_mul_f32 v253, v253, v250\n\t"
    "v_accvgpr_write_b32 a37, v253\n\t"
    "v_accvgpr_read_b32 v252, a38\n\t"
    "v_mul_f32 v252, v252, v250\n\t"
    "v_accvgpr_write_b32 a38, v252\n\t"
    "v_accvgpr_read_b32 v253, a39\n\t"
    "v_mul_f32 v253, v253, v250\n\t"
    "v_accvgpr_write_b32 a39, v253\n\t"
    "v_accvgpr_read_b32 v252, a40\n\t"
    "v_mul_f32 v252, v252, v250\n\t"
    "v_accvgpr_write_b32 a40, v252\n\t"
    "v_accvgpr_read_b32 v253, a41\n\t"
    "v_mul_f32 v253, v253, v250\n\t"
    "v_accvgpr_write_b32 a41, v253\n\t"
    "v_accvgpr_read_b32 v252, a42\n\t"
    "v_mul_f32 v252, v252, v250\n\t"
    "v_accvgpr_write_b32 a42, v252\n\t"
    "v_accvgpr_read_b32 v253, a43\n\t"
    "v_mul_f32 v253, v253, v250\n\t"
    "v_accvgpr_write_b32 a43, v253\n\t"
    "v_accvgpr_read_b32 v252, a44\n\t"
    "v_mul_f32 v252, v252, v250\n\t"
    "v_accvgpr_write_b32 a44, v252\n\t"
    "v_accvgpr_read_b32 v253, a45\n\t"
    "v_mul_f32 v253, v253, v250\n\t"
    "v_accvgpr_write_b32 a45, v253\n\t"
    "v_accvgpr_read_b32 v252, a46\n\t"
    "v_mul_f32 v252, v252, v250\n\t"
    "v_accvgpr_write_b32 a46, v252\n\t"
    "v_accvgpr_read_b32 v253, a47\n\t"
    "v_mul_f32 v253, v253, v250\n\t"
    "v_accvgpr_write_b32 a47, v253\n\t"
    "v_accvgpr_read_b32 v252, a48\n\t"
    "v_mul_f32 v252, v252, v251\n\t"
    "v_accvgpr_write_b32 a48, v252\n\t"
    "v_accvgpr_read_b32 v253, a49\n\t"
    "v_mul_f32 v253, v253, v251\n\t"
    "v_accvgpr_write_b32 a49, v253\n\t"
    "v_accvgpr_read_b32 v252, a50\n\t"
    "v_mul_f32 v252, v252, v251\n\t"
    "v_accvgpr_write_b32 a50, v252\n\t"
    "v_accvgpr_read_b32 v253, a51\n\t"
    "v_mul_f32 v253, v253, v251\n\t"
    "v_accvgpr_write_b32 a51, v253\n\t"
    "v_accvgpr_read_b32 v252, a52\n\t"
    "v_mul_f32 v252, v252, v251\n\t"
    "v_accvgpr_write_b32 a52, v252\n\t"
    "v_accvgpr_read_b32 v253, a53\n\t"
    "v_mul_f32 v253, v253, v251\n\t"
    "v_accvgpr_write_b32 a53, v253\n\t"
    "v_accvgpr_read_b32 v252, a54\n\t"
    "v_mul_f32 v252, v252, v251\n\t"
    "v_accvgpr_write_b32 a54, v252\n\t"
    "v_accvgpr_read_b32 v253, a55\n\t"
    "v_mul_f32 v253, v253, v251\n\t"
    "v_accvgpr_write_b32 a55, v253\n\t"
    "v_accvgpr_read_b32 v252, a56\n\t"
    "v_mul_f32 v252, v252, v251\n\t"
    "v_accvgpr_write_b32 a56, v252\n\t"
    "v_accvgpr_read_b32 v253, a57\n\t"
    "v_mul_f32 v253, v253, v251\n\t"
    "v_accvgpr_write_b32 a57, v253\n\t"
    "v_accvgpr_read_b32 v252, a58\n\t"
    "v_mul_f32 v252, v252, v251\n\t"
    "v_accvgpr_write_b32 a58, v252\n\t"
    "v_accvgpr_read_b32 v253, a59\n\t"
    "v_mul_f32 v253, v253, v251\n\t"
    "v_accvgpr_write_b32 a59, v253\n\t"
    "v_accvgpr_read_b32 v252, a60\n\t"
    "v_mul_f32 v252, v252, v251\n\t"
    "v_accvgpr_write_b32 a60, v252\n\t"
    "v_accvgpr_read_b32 v253, a61\n\t"
    "v_mul_f32 v253, v253, v251\n\t"
    "v_accvgpr_write_b32 a61, v253\n\t"
    "v_accvgpr_read_b32 v252, a62\n\t"
    "v_mul_f32 v252, v252, v251\n\t"
    "v_accvgpr_write_b32 a62, v252\n\t"
    "v_accvgpr_read_b32 v253, a63\n\t"
    "v_mul_f32 v253, v253, v251\n\t"
    "v_accvgpr_write_b32 a63, v253\n\t"
    "v_accvgpr_read_b32 v252, a64\n\t"
    "v_mul_f32 v252, v252, v250\n\t"
    "v_accvgpr_write_b32 a64, v252\n\t"
    "v_accvgpr_read_b32 v253, a65\n\t"
    "v_mul_f32 v253, v253, v250\n\t"
    "v_accvgpr_write_b32 a65, v253\n\t"
    "v_accvgpr_read_b32 v252, a66\n\t"
    "v_mul_f32 v252, v252, v250\n\t"
    "v_accvgpr_write_b32 a66, v252\n\t"
    "v_accvgpr_read_b32 v253, a67\n\t"
    "v_mul_f32 v253, v253, v250\n\t"
    "v_accvgpr_write_b32 a67, v253\n\t"
    "v_accvgpr_read_b32 v252, a68\n\t"
    "v_mul_f32 v252, v252, v250\n\t"
    "v_accvgpr_write_b32 a68, v252\n\t"
    "v_accvgpr_read_b32 v253, a69\n\t"
    "v_mul_f32 v253, v253, v250\n\t"
    "v_accvgpr_write_b32 a69, v253\n\t"
    "v_accvgpr_read_b32 v252, a70\n\t"
    "v_mul_f32 v252, v252, v250\n\t"
    "v_accvgpr_write_b32 a70, v252\n\t"
    "v_accvgpr_read_b32 v253, a71\n\t"
    "v_mul_f32 v253, v253, v250\n\t"
    "v_accvgpr_write_b32 a71, v253\n\t"
    "v_accvgpr_read_b32 v252, a72\n\t"
    "v_mul_f32 v252, v252, v250\n\t"
    "v_accvgpr_write_b32 a72, v252\n\t"
    "v_accvgpr_read_b32 v253, a73\n\t"
    "v_mul_f32 v253, v253, v250\n\t"
    "v_accvgpr_write_b32 a73, v253\n\t"
    "v_accvgpr_read_b32 v252, a74\n\t"
    "v_mul_f32 v252, v252, v250\n\t"
    "v_accvgpr_write_b32 a74, v252\n\t"
    "v_accvgpr_read_b32 v253, a75\n\t"
    "v_mul_f32 v253, v253, v250\n\t"
    "v_accvgpr_write_b32 a75, v253\n\t"
    "v_accvgpr_read_b32 v252, a76\n\t"
    "v_mul_f32 v252, v252, v250\n\t"
    "v_accvgpr_write_b32 a76, v252\n\t"
    "v_accvgpr_read_b32 v253, a77\n\t"
    "v_mul_f32 v253, v253, v250\n\t"
    "v_accvgpr_write_b32 a77, v253\n\t"
    "v_accvgpr_read_b32 v252, a78\n\t"
    "v_mul_f32 v252, v252, v250\n\t"
    "v_accvgpr_write_b32 a78, v252\n\t"
    "v_accvgpr_read_b32 v253, a79\n\t"
    "v_mul_f32 v253, v253, v250\n\t"
    "v_accvgpr_write_b32 a79, v253\n\t"
    "v_accvgpr_read_b32 v252, a80\n\t"
    "v_mul_f32 v252, v252, v251\n\t"
    "v_accvgpr_write_b32 a80, v252\n\t"
    "v_accvgpr_read_b32 v253, a81\n\t"
    "v_mul_f32 v253, v253, v251\n\t"
    "v_accvgpr_write_b32 a81, v253\n\t"
    "v_accvgpr_read_b32 v252, a82\n\t"
    "v_mul_f32 v252, v252, v251\n\t"
    "v_accvgpr_write_b32 a82, v252\n\t"
    "v_accvgpr_read_b32 v253, a83\n\t"
    "v_mul_f32 v253, v253, v251\n\t"
    "v_accvgpr_write_b32 a83, v253\n\t"
    "v_accvgpr_read_b32 v252, a84\n\t"
    "v_mul_f32 v252, v252, v251\n\t"
    "v_accvgpr_write_b32 a84, v252\n\t"
    "v_accvgpr_read_b32 v253, a85\n\t"
    "v_mul_f32 v253, v253, v251\n\t"
    "v_accvgpr_write_b32 a85, v253\n\t"
    "v_accvgpr_read_b32 v252, a86\n\t"
    "v_mul_f32 v252, v252, v251\n\t"
    "v_accvgpr_write_b32 a86, v252\n\t"
    "v_accvgpr_read_b32 v253, a87\n\t"
    "v_mul_f32 v253, v253, v251\n\t"
    "v_accvgpr_write_b32 a87, v253\n\t"
    "v_accvgpr_read_b32 v252, a88\n\t"
    "v_mul_f32 v252, v252, v251\n\t"
    "v_accvgpr_write_b32 a88, v252\n\t"
    "v_accvgpr_read_b32 v253, a89\n\t"
    "v_mul_f32 v253, v253, v251\n\t"
    "v_accvgpr_write_b32 a89, v253\n\t"
    "v_accvgpr_read_b32 v252, a90\n\t"
    "v_mul_f32 v252, v252, v251\n\t"
    "v_accvgpr_write_b32 a90, v252\n\t"
    "v_accvgpr_read_b32 v253, a91\n\t"
    "v_mul_f32 v253, v253, v251\n\t"
    "v_accvgpr_write_b32 a91, v253\n\t"
    "v_accvgpr_read_b32 v252, a92\n\t"
    "v_mul_f32 v252, v252, v251\n\t"
    "v_accvgpr_write_b32 a92, v252\n\t"
    "v_accvgpr_read_b32 v253, a93\n\t"
    "v_mul_f32 v253, v253, v251\n\t"
    "v_accvgpr_write_b32 a93, v253\n\t"
    "v_accvgpr_read_b32 v252, a94\n\t"
    "v_mul_f32 v252, v252, v251\n\t"
    "v_accvgpr_write_b32 a94, v252\n\t"
    "v_accvgpr_read_b32 v253, a95\n\t"
    "v_mul_f32 v253, v253, v251\n\t"
    "v_accvgpr_write_b32 a95, v253\n\t"
    "v_accvgpr_read_b32 v252, a96\n\t"
    "v_mul_f32 v252, v252, v250\n\t"
    "v_accvgpr_write_b32 a96, v252\n\t"
    "v_accvgpr_read_b32 v253, a97\n\t"
    "v_mul_f32 v253, v253, v250\n\t"
    "v_accvgpr_write_b32 a97, v253\n\t"
    "v_accvgpr_read_b32 v252, a98\n\t"
    "v_mul_f32 v252, v252, v250\n\t"
    "v_accvgpr_write_b32 a98, v252\n\t"
    "v_accvgpr_read_b32 v253, a99\n\t"
    "v_mul_f32 v253, v253, v250\n\t"
    "v_accvgpr_write_b32 a99, v253\n\t"
    "v_accvgpr_read_b32 v252, a100\n\t"
    "v_mul_f32 v252, v252, v250\n\t"
    "v_accvgpr_write_b32 a100, v252\n\t"
    "v_accvgpr_read_b32 v253, a101\n\t"
    "v_mul_f32 v253, v253, v250\n\t"
    "v_accvgpr_write_b32 a101, v253\n\t"
    "v_accvgpr_read_b32 v252, a102\n\t"
    "v_mul_f32 v252, v252, v250\n\t"
    "v_accvgpr_write_b32 a102, v252\n\t"
    "v_accvgpr_read_b32 v253, a103\n\t"
    "v_mul_f32 v253, v253, v250\n\t"
    "v_accvgpr_write_b32 a103, v253\n\t"
    "v_accvgpr_read_b32 v252, a104\n\t"
    "v_mul_f32 v252, v252, v250\n\t"
    "v_accvgpr_write_b32 a104, v252\n\t"
    "v_accvgpr_read_b32 v253, a105\n\t"
    "v_mul_f32 v253, v253, v250\n\t"
    "v_accvgpr_write_b32 a105, v253\n\t"
    "v_accvgpr_read_b32 v252, a106\n\t"
    "v_mul_f32 v252, v252, v250\n\t"
    "v_accvgpr_write_b32 a106, v252\n\t"
    "v_accvgpr_read_b32 v253, a107\n\t"
    "v_mul_f32 v253, v253, v250\n\t"
    "v_accvgpr_write_b32 a107, v253\n\t"
    "v_accvgpr_read_b32 v252, a108\n\t"
    "v_mul_f32 v252, v252, v250\n\t"
    "v_accvgpr_write_b32 a108, v252\n\t"
    "v_accvgpr_read_b32 v253, a109\n\t"
    "v_mul_f32 v253, v253, v250\n\t"
    "v_accvgpr_write_b32 a109, v253\n\t"
    "v_accvgpr_read_b32 v252, a110\n\t"
    "v_mul_f32 v252, v252, v250\n\t"
    "v_accvgpr_write_b32 a110, v252\n\t"
    "v_accvgpr_read_b32 v253, a111\n\t"
    "v_mul_f32 v253, v253, v250\n\t"
    "v_accvgpr_write_b32 a111, v253\n\t"
    "v_accvgpr_read_b32 v252, a112\n\t"
    "v_mul_f32 v252, v252, v251\n\t"
    "v_accvgpr_write_b32 a112, v252\n\t"
    "v_accvgpr_read_b32 v253, a113\n\t"
    "v_mul_f32 v253, v253, v251\n\t"
    "v_accvgpr_write_b32 a113, v253\n\t"
    "v_accvgpr_read_b32 v252, a114\n\t"
    "v_mul_f32 v252, v252, v251\n\t"
    "v_accvgpr_write_b32 a114, v252\n\t"
    "v_accvgpr_read_b32 v253, a115\n\t"
    "v_mul_f32 v253, v253, v251\n\t"
    "v_accvgpr_write_b32 a115, v253\n\t"
    "v_accvgpr_read_b32 v252, a116\n\t"
    "v_mul_f32 v252, v252, v251\n\t"
    "v_accvgpr_write_b32 a116, v252\n\t"
    "v_accvgpr_read_b32 v253, a117\n\t"
    "v_mul_f32 v253, v253, v251\n\t"
    "v_accvgpr_write_b32 a117, v253\n\t"
    "v_accvgpr_read_b32 v252, a118\n\t"
    "v_mul_f32 v252, v252, v251\n\t"
    "v_accvgpr_write_b32 a118, v252\n\t"
    "v_accvgpr_read_b32 v253, a119\n\t"
    "v_mul_f32 v253, v253, v251\n\t"
    "v_accvgpr_write_b32 a119, v253\n\t"
    "v_accvgpr_read_b32 v252, a120\n\t"
    "v_mul_f32 v252, v252, v251\n\t"
    "v_accvgpr_write_b32 a120, v252\n\t"
    "v_accvgpr_read_b32 v253, a121\n\t"
    "v_mul_f32 v253, v253, v251\n\t"
    "v_accvgpr_write_b32 a121, v253\n\t"
    "v_accvgpr_read_b32 v252, a122\n\t"
    "v_mul_f32 v252, v252, v251\n\t"
    "v_accvgpr_write_b32 a122, v252\n\t"
    "v_accvgpr_read_b32 v253, a123\n\t"
    "v_mul_f32 v253, v253, v251\n\t"
    "v_accvgpr_write_b32 a123, v253\n\t"
    "v_accvgpr_read_b32 v252, a124\n\t"
    "v_mul_f32 v252, v252, v251\n\t"
    "v_accvgpr_write_b32 a124, v252\n\t"
    "v_accvgpr_read_b32 v253, a125\n\t"
    "v_mul_f32 v253, v253, v251\n\t"
    "v_accvgpr_write_b32 a125, v253\n\t"
    "v_accvgpr_read_b32 v252, a126\n\t"
    "v_mul_f32 v252, v252, v251\n\t"
    "v_accvgpr_write_b32 a126, v252\n\t"
    "v_accvgpr_read_b32 v253, a127\n\t"
    "v_mul_f32 v253, v253, v251\n\t"
    "v_accvgpr_write_b32 a127, v253\n\t"
    "s_mov_b32 s46, 0\n\t"
    "s_nop 7\n\t"
    "s_branch RARE2R_2%=\n\t"
    "RARE1_3%=:\n\t"
    "s_nop 7\n\t"
    "s_nop 7\n\t"
    "v_mov_b32 v252, v248\n\t"
    "s_nop 1\n\t"
    "v_permlane32_swap_b32 v248, v252\n\t"
    "v_max_f32 v248, v248, v252\n\t"
    "v_max_f32 v253, 0, v248\n\t"
    "v_add_f32 v242, v242, v253\n\t"
    "v_sub_f32 v250, 0, v253\n\t"
    "v_exp_f32 v250, v250\n\t"
    "s_nop 0\n\t"
    "v_mul_f32 v244, v244, v250\n\t"
    "v_mul_f32 v245, v245, v250\n\t"
    "v_sub_f32 v64, v64, v253\n\t"
    "v_sub_f32 v65, v65, v253\n\t"
    "v_sub_f32 v66, v66, v253\n\t"
    "v_sub_f32 v67, v67, v253\n\t"
    "v_sub_f32 v68, v68, v253\n\t"
    "v_sub_f32 v69, v69, v253\n\t"
    "v_sub_f32 v70, v70, v253\n\t"
    "v_sub_f32 v71, v71, v253\n\t"
    "v_sub_f32 v72, v72, v253\n\t"
    "v_sub_f32 v73, v73, v253\n\t"
    "v_sub_f32 v74, v74, v253\n\t"
    "v_sub_f32 v75, v75, v253\n\t"
    "v_sub_f32 v76, v76, v253\n\t"
    "v_sub_f32 v77, v77, v253\n\t"
    "v_sub_f32 v78, v78, v253\n\t"
    "v_sub_f32 v79, v79, v253\n\t"
    "v_sub_f32 v96, v96, v253\n\t"
    "v_sub_f32 v97, v97, v253\n\t"
    "v_sub_f32 v98, v98, v253\n\t"
    "v_sub_f32 v99, v99, v253\n\t"
    "v_sub_f32 v100, v100, v253\n\t"
    "v_sub_f32 v101, v101, v253\n\t"
    "v_sub_f32 v102, v102, v253\n\t"
    "v_sub_f32 v103, v103, v253\n\t"
    "v_sub_f32 v104, v104, v253\n\t"
    "v_sub_f32 v105, v105, v253\n\t"
    "v_sub_f32 v106, v106, v253\n\t"
    "v_sub_f32 v107, v107, v253\n\t"
    "v_sub_f32 v108, v108, v253\n\t"
    "v_sub_f32 v109, v109, v253\n\t"
    "v_sub_f32 v110, v110, v253\n\t"
    "v_sub_f32 v111, v111, v253\n\t"
    "v_sub_f32 v252, 0, v242\n\t"
    "v_mov_b32 v192, v252\n\t"
    "v_mov_b32 v193, v252\n\t"
    "v_mov_b32 v194, v252\n\t"
    "v_mov_b32 v195, v252\n\t"
    "v_mov_b32 v196, v252\n\t"
    "v_mov_b32 v197, v252\n\t"
    "v_mov_b32 v198, v252\n\t"
    "v_mov_b32 v199, v252\n\t"
    "v_mov_b32 v200, v252\n\t"
    "v_mov_b32 v201, v252\n\t"
    "v_mov_b32 v202, v252\n\t"
    "v_mov_b32 v203, v252\n\t"
    "v_mov_b32 v204, v252\n\t"
    "v_mov_b32 v205, v252\n\t"
    "v_mov_b32 v206, v252\n\t"
    "v_mov_b32 v207, v252\n\t"
    "v_mov_b32 v252, v249\n\t"
    "s_nop 1\n\t"
    "v_permlane32_swap_b32 v249, v252\n\t"
    "v_max_f32 v249, v249, v252\n\t"
    "v_max_f32 v253, 0, v249\n\t"
    "v_add_f32 v243, v243, v253\n\t"
    "v_sub_f32 v251, 0, v253\n\t"
    "v_exp_f32 v251, v251\n\t"
    "s_nop 0\n\t"
    "v_mul_f32 v246, v246, v251\n\t"
    "v_mul_f32 v247, v247, v251\n\t"
    "v_sub_f32 v80, v80, v253\n\t"
    "v_sub_f32 v81, v81, v253\n\t"
    "v_sub_f32 v82, v82, v253\n\t"
    "v_sub_f32 v83, v83, v253\n\t"
    "v_sub_f32 v84, v84, v253\n\t"
    "v_sub_f32 v85, v85, v253\n\t"
    "v_sub_f32 v86, v86, v253\n\t"
    "v_sub_f32 v87, v87, v253\n\t"
    "v_sub_f32 v88, v88, v253\n\t"
    "v_sub_f32 v89, v89, v253\n\t"
    "v_sub_f32 v90, v90, v253\n\t"
    "v_sub_f32 v91, v91, v253\n\t"
    "v_sub_f32 v92, v92, v253\n\t"
    "v_sub_f32 v93, v93, v253\n\t"
    "v_sub_f32 v94, v94, v253\n\t"
    "v_sub_f32 v95, v95, v253\n\t"
    "v_sub_f32 v112, v112, v253\n\t"
    "v_sub_f32 v113, v113, v253\n\t"
    "v_sub_f32 v114, v114, v253\n\t"
    "v_sub_f32 v115, v115, v253\n\t"
    "v_sub_f32 v116, v116, v253\n\t"
    "v_sub_f32 v117, v117, v253\n\t"
    "v_sub_f32 v118, v118, v253\n\t"
    "v_sub_f32 v119, v119, v253\n\t"
    "v_sub_f32 v120, v120, v253\n\t"
    "v_sub_f32 v121, v121, v253\n\t"
    "v_sub_f32 v122, v122, v253\n\t"
    "v_sub_f32 v123, v123, v253\n\t"
    "v_sub_f32 v124, v124, v253\n\t"
    "v_sub_f32 v125, v125, v253\n\t"
    "v_sub_f32 v126, v126, v253\n\t"
    "v_sub_f32 v127, v127, v253\n\t"
    "v_sub_f32 v252, 0, v243\n\t"
    "v_mov_b32 v208, v252\n\t"
    "v_mov_b32 v209, v252\n\t"
    "v_mov_b32 v210, v252\n\t"
    "v_mov_b32 v211, v252\n\t"
    "v_mov_b32 v212, v252\n\t"
    "v_mov_b32 v213, v252\n\t"
    "v_mov_b32 v214, v252\n\t"
    "v_mov_b32 v215, v252\n\t"
    "v_mov_b32 v216, v252\n\t"
    "v_mov_b32 v217, v252\n\t"
    "v_mov_b32 v218, v252\n\t"
    "v_mov_b32 v219, v252\n\t"
    "v_mov_b32 v220, v252\n\t"
    "v_mov_b32 v221, v252\n\t"
    "v_mov_b32 v222, v252\n\t"
    "v_mov_b32 v223, v252\n\t"
    "s_mov_b32 s46, 1\n\t"
    "s_nop 7\n\t"
    "s_branch RARE1R_3%=\n\t"
    "RARE2_3%=:\n\t"
    "s_nop 7\n\t"
    "s_nop 7\n\t"
    "v_accvgpr_read_b32 v252, a0\n\t"
    "v_mul_f32 v252, v252, v250\n\t"
    "v_accvgpr_write_b32 a0, v252\n\t"
    "v_accvgpr_read_b32 v253, a1\n\t"
    "v_mul_f32 v253, v253, v250\n\t"
    "v_accvgpr_write_b32 a1, v253\n\t"
    "v_accvgpr_read_b32 v252, a2\n\t"
    "v_mul_f32 v252, v252, v250\n\t"
    "v_accvgpr_write_b32 a2, v252\n\t"
    "v_accvgpr_read_b32 v253, a3\n\t"
    "v_mul_f32 v253, v253, v250\n\t"
    "v_accvgpr_write_b32 a3, v253\n\t"
    "v_accvgpr_read_b32 v252, a4\n\t"
    "v_mul_f32 v252, v252, v250\n\t"
    "v_accvgpr_write_b32 a4, v252\n\t"
    "v_accvgpr_read_b32 v253, a5\n\t"
    "v_mul_f32 v253, v253, v250\n\t"
    "v_accvgpr_write_b32 a5, v253\n\t"
    "v_accvgpr_read_b32 v252, a6\n\t"
    "v_mul_f32 v252, v252, v250\n\t"
    "v_accvgpr_write_b32 a6, v252\n\t"
    "v_accvgpr_read_b32 v253, a7\n\t"
    "v_mul_f32 v253, v253, v250\n\t"
    "v_accvgpr_write_b32 a7, v253\n\t"
    "v_accvgpr_read_b32 v252, a8\n\t"
    "v_mul_f32 v252, v252, v250\n\t"
    "v_accvgpr_write_b32 a8, v252\n\t"
    "v_accvgpr_read_b32 v253, a9\n\t"
    "v_mul_f32 v253, v253, v250\n\t"
    "v_accvgpr_write_b32 a9, v253\n\t"
    "v_accvgpr_read_b32 v252, a10\n\t"
    "v_mul_f32 v252, v252, v250\n\t"
    "v_accvgpr_write_b32 a10, v252\n\t"
    "v_accvgpr_read_b32 v253, a11\n\t"
    "v_mul_f32 v253, v253, v250\n\t"
    "v_accvgpr_write_b32 a11, v253\n\t"
    "v_accvgpr_read_b32 v252, a12\n\t"
    "v_mul_f32 v252, v252, v250\n\t"
    "v_accvgpr_write_b32 a12, v252\n\t"
    "v_accvgpr_read_b32 v253, a13\n\t"
    "v_mul_f32 v253, v253, v250\n\t"
    "v_accvgpr_write_b32 a13, v253\n\t"
    "v_accvgpr_read_b32 v252, a14\n\t"
    "v_mul_f32 v252, v252, v250\n\t"
    "v_accvgpr_write_b32 a14, v252\n\t"
    "v_accvgpr_read_b32 v253, a15\n\t"
    "v_mul_f32 v253, v253, v250\n\t"
    "v_accvgpr_write_b32 a15, v253\n\t"
    "v_accvgpr_read_b32 v252, a16\n\t"
    "v_mul_f32 v252, v252, v251\n\t"
    "v_accvgpr_write_b32 a16, v252\n\t"
    "v_accvgpr_read_b32 v253, a17\n\t"
    "v_mul_f32 v253, v253, v251\n\t"
    "v_accvgpr_write_b32 a17, v253\n\t"
    "v_accvgpr_read_b32 v252, a18\n\t"
    "v_mul_f32 v252, v252, v251\n\t"
    "v_accvgpr_write_b32 a18, v252\n\t"
    "v_accvgpr_read_b32 v253, a19\n\t"
    "v_mul_f32 v253, v253, v251\n\t"
    "v_accvgpr_write_b32 a19, v253\n\t"
    "v_accvgpr_read_b32 v252, a20\n\t"
    "v_mul_f32 v252, v252, v251\n\t"
    "v_accvgpr_write_b32 a20, v252\n\t"
    "v_accvgpr_read_b32 v253, a21\n\t"
    "v_mul_f32 v253, v253, v251\n\t"
    "v_accvgpr_write_b32 a21, v253\n\t"
    "v_accvgpr_read_b32 v252, a22\n\t"
    "v_mul_f32 v252, v252, v251\n\t"
    "v_accvgpr_write_b32 a22, v252\n\t"
    "v_accvgpr_read_b32 v253, a23\n\t"
    "v_mul_f32 v253, v253, v251\n\t"
    "v_accvgpr_write_b32 a23, v253\n\t"
    "v_accvgpr_read_b32 v252, a24\n\t"
    "v_mul_f32 v252, v252, v251\n\t"
    "v_accvgpr_write_b32 a24, v252\n\t"
    "v_accvgpr_read_b32 v253, a25\n\t"
    "v_mul_f32 v253, v253, v251\n\t"
    "v_accvgpr_write_b32 a25, v253\n\t"
    "v_accvgpr_read_b32 v252, a26\n\t"
    "v_mul_f32 v252, v252, v251\n\t"
    "v_accvgpr_write_b32 a26, v252\n\t"
    "v_accvgpr_read_b32 v253, a27\n\t"
    "v_mul_f32 v253, v253, v251\n\t"
    "v_accvgpr_write_b32 a27, v253\n\t"
    "v_accvgpr_read_b32 v252, a28\n\t"
    "v_mul_f32 v252, v252, v251\n\t"
    "v_accvgpr_write_b32 a28, v252\n\t"
    "v_accvgpr_read_b32 v253, a29\n\t"
    "v_mul_f32 v253, v253, v251\n\t"
    "v_accvgpr_write_b32 a29, v253\n\t"
    "v_accvgpr_read_b32 v252, a30\n\t"
    "v_mul_f32 v252, v252, v251\n\t"
    "v_accvgpr_write_b32 a30, v252\n\t"
    "v_accvgpr_read_b32 v253, a31\n\t"
    "v_mul_f32 v253, v253, v251\n\t"
    "v_accvgpr_write_b32 a31, v253\n\t"
    "v_accvgpr_read_b32 v252, a32\n\t"
    "v_mul_f32 v252, v252, v250\n\t"
    "v_accvgpr_write_b32 a32, v252\n\t"
    "v_accvgpr_read_b32 v253, a33\n\t"
    "v_mul_f32 v253, v253, v250\n\t"
    "v_accvgpr_write_b32 a33, v253\n\t"
    "v_accvgpr_read_b32 v252, a34\n\t"
    "v_mul_f32 v252, v252, v250\n\t"
    "v_accvgpr_write_b32 a34, v252\n\t"
    "v_accvgpr_read_b32 v253, a35\n\t"
    "v_mul_f32 v253, v253, v250\n\t"
    "v_accvgpr_write_b32 a35, v253\n\t"
    "v_accvgpr_read_b32 v252, a36\n\t"
    "v_mul_f32 v252, v252, v250\n\t"
    "v_accvgpr_write_b32 a36, v252\n\t"
    "v_accvgpr_read_b32 v253, a37\n\t"
    "v_mul_f32 v253, v253, v250\n\t"
    "v_accvgpr_write_b32 a37, v253\n\t"
    "v_accvgpr_read_b32 v252, a38\n\t"
    "v_mul_f32 v252, v252, v250\n\t"
    "v_accvgpr_write_b32 a38, v252\n\t"
    "v_accvgpr_read_b32 v253, a39\n\t"
    "v_mul_f32 v253, v253, v250\n\t"
    "v_accvgpr_write_b32 a39, v253\n\t"
    "v_accvgpr_read_b32 v252, a40\n\t"
    "v_mul_f32 v252, v252, v250\n\t"
    "v_accvgpr_write_b32 a40, v252\n\t"
    "v_accvgpr_read_b32 v253, a41\n\t"
    "v_mul_f32 v253, v253, v250\n\t"
    "v_accvgpr_write_b32 a41, v253\n\t"
    "v_accvgpr_read_b32 v252, a42\n\t"
    "v_mul_f32 v252, v252, v250\n\t"
    "v_accvgpr_write_b32 a42, v252\n\t"
    "v_accvgpr_read_b32 v253, a43\n\t"
    "v_mul_f32 v253, v253, v250\n\t"
    "v_accvgpr_write_b32 a43, v253\n\t"
    "v_accvgpr_read_b32 v252, a44\n\t"
    "v_mul_f32 v252, v252, v250\n\t"
    "v_accvgpr_write_b32 a44, v252\n\t"
    "v_accvgpr_read_b32 v253, a45\n\t"
    "v_mul_f32 v253, v253, v250\n\t"
    "v_accvgpr_write_b32 a45, v253\n\t"
    "v_accvgpr_read_b32 v252, a46\n\t"
    "v_mul_f32 v252, v252, v250\n\t"
    "v_accvgpr_write_b32 a46, v252\n\t"
    "v_accvgpr_read_b32 v253, a47\n\t"
    "v_mul_f32 v253, v253, v250\n\t"
    "v_accvgpr_write_b32 a47, v253\n\t"
    "v_accvgpr_read_b32 v252, a48\n\t"
    "v_mul_f32 v252, v252, v251\n\t"
    "v_accvgpr_write_b32 a48, v252\n\t"
    "v_accvgpr_read_b32 v253, a49\n\t"
    "v_mul_f32 v253, v253, v251\n\t"
    "v_accvgpr_write_b32 a49, v253\n\t"
    "v_accvgpr_read_b32 v252, a50\n\t"
    "v_mul_f32 v252, v252, v251\n\t"
    "v_accvgpr_write_b32 a50, v252\n\t"
    "v_accvgpr_read_b32 v253, a51\n\t"
    "v_mul_f32 v253, v253, v251\n\t"
    "v_accvgpr_write_b32 a51, v253\n\t"
    "v_accvgpr_read_b32 v252, a52\n\t"
    "v_mul_f32 v252, v252, v251\n\t"
    "v_accvgpr_write_b32 a52, v252\n\t"
    "v_accvgpr_read_b32 v253, a53\n\t"
    "v_mul_f32 v253, v253, v251\n\t"
    "v_accvgpr_write_b32 a53, v253\n\t"
    "v_accvgpr_read_b32 v252, a54\n\t"
    "v_mul_f32 v252, v252, v251\n\t"
    "v_accvgpr_write_b32 a54, v252\n\t"
    "v_accvgpr_read_b32 v253, a55\n\t"
    "v_mul_f32 v253, v253, v251\n\t"
    "v_accvgpr_write_b32 a55, v253\n\t"
    "v_accvgpr_read_b32 v252, a56\n\t"
    "v_mul_f32 v252, v252, v251\n\t"
    "v_accvgpr_write_b32 a56, v252\n\t"
    "v_accvgpr_read_b32 v253, a57\n\t"
    "v_mul_f32 v253, v253, v251\n\t"
    "v_accvgpr_write_b32 a57, v253\n\t"
    "v_accvgpr_read_b32 v252, a58\n\t"
    "v_mul_f32 v252, v252, v251\n\t"
    "v_accvgpr_write_b32 a58, v252\n\t"
    "v_accvgpr_read_b32 v253, a59\n\t"
    "v_mul_f32 v253, v253, v251\n\t"
    "v_accvgpr_write_b32 a59, v253\n\t"
    "v_accvgpr_read_b32 v252, a60\n\t"
    "v_mul_f32 v252, v252, v251\n\t"
    "v_accvgpr_write_b32 a60, v252\n\t"
    "v_accvgpr_read_b32 v253, a61\n\t"
    "v_mul_f32 v253, v253, v251\n\t"
    "v_accvgpr_write_b32 a61, v253\n\t"
    "v_accvgpr_read_b32 v252, a62\n\t"
    "v_mul_f32 v252, v252, v251\n\t"
    "v_accvgpr_write_b32 a62, v252\n\t"
    "v_accvgpr_read_b32 v253, a63\n\t"
    "v_mul_f32 v253, v253, v251\n\t"
    "v_accvgpr_write_b32 a63, v253\n\t"
    "v_accvgpr_read_b32 v252, a64\n\t"
    "v_mul_f32 v252, v252, v250\n\t"
    "v_accvgpr_write_b32 a64, v252\n\t"
    "v_accvgpr_read_b32 v253, a65\n\t"
    "v_mul_f32 v253, v253, v250\n\t"
    "v_accvgpr_write_b32 a65, v253\n\t"
    "v_accvgpr_read_b32 v252, a66\n\t"
    "v_mul_f32 v252, v252, v250\n\t"
    "v_accvgpr_write_b32 a66, v252\n\t"
    "v_accvgpr_read_b32 v253, a67\n\t"
    "v_mul_f32 v253, v253, v250\n\t"
    "v_accvgpr_write_b32 a67, v253\n\t"
    "v_accvgpr_read_b32 v252, a68\n\t"
    "v_mul_f32 v252, v252, v250\n\t"
    "v_accvgpr_write_b32 a68, v252\n\t"
    "v_accvgpr_read_b32 v253, a69\n\t"
    "v_mul_f32 v253, v253, v250\n\t"
    "v_accvgpr_write_b32 a69, v253\n\t"
    "v_accvgpr_read_b32 v252, a70\n\t"
    "v_mul_f32 v252, v252, v250\n\t"
    "v_accvgpr_write_b32 a70, v252\n\t"
    "v_accvgpr_read_b32 v253, a71\n\t"
    "v_mul_f32 v253, v253, v250\n\t"
    "v_accvgpr_write_b32 a71, v253\n\t"
    "v_accvgpr_read_b32 v252, a72\n\t"
    "v_mul_f32 v252, v252, v250\n\t"
    "v_accvgpr_write_b32 a72, v252\n\t"
    "v_accvgpr_read_b32 v253, a73\n\t"
    "v_mul_f32 v253, v253, v250\n\t"
    "v_accvgpr_write_b32 a73, v253\n\t"
    "v_accvgpr_read_b32 v252, a74\n\t"
    "v_mul_f32 v252, v252, v250\n\t"
    "v_accvgpr_write_b32 a74, v252\n\t"
    "v_accvgpr_read_b32 v253, a75\n\t"
    "v_mul_f32 v253, v253, v250\n\t"
    "v_accvgpr_write_b32 a75, v253\n\t"
    "v_accvgpr_read_b32 v252, a76\n\t"
    "v_mul_f32 v252, v252, v250\n\t"
    "v_accvgpr_write_b32 a76, v252\n\t"
    "v_accvgpr_read_b32 v253, a77\n\t"
    "v_mul_f32 v253, v253, v250\n\t"
    "v_accvgpr_write_b32 a77, v253\n\t"
    "v_accvgpr_read_b32 v252, a78\n\t"
    "v_mul_f32 v252, v252, v250\n\t"
    "v_accvgpr_write_b32 a78, v252\n\t"
    "v_accvgpr_read_b32 v253, a79\n\t"
    "v_mul_f32 v253, v253, v250\n\t"
    "v_accvgpr_write_b32 a79, v253\n\t"
    "v_accvgpr_read_b32 v252, a80\n\t"
    "v_mul_f32 v252, v252, v251\n\t"
    "v_accvgpr_write_b32 a80, v252\n\t"
    "v_accvgpr_read_b32 v253, a81\n\t"
    "v_mul_f32 v253, v253, v251\n\t"
    "v_accvgpr_write_b32 a81, v253\n\t"
    "v_accvgpr_read_b32 v252, a82\n\t"
    "v_mul_f32 v252, v252, v251\n\t"
    "v_accvgpr_write_b32 a82, v252\n\t"
    "v_accvgpr_read_b32 v253, a83\n\t"
    "v_mul_f32 v253, v253, v251\n\t"
    "v_accvgpr_write_b32 a83, v253\n\t"
    "v_accvgpr_read_b32 v252, a84\n\t"
    "v_mul_f32 v252, v252, v251\n\t"
    "v_accvgpr_write_b32 a84, v252\n\t"
    "v_accvgpr_read_b32 v253, a85\n\t"
    "v_mul_f32 v253, v253, v251\n\t"
    "v_accvgpr_write_b32 a85, v253\n\t"
    "v_accvgpr_read_b32 v252, a86\n\t"
    "v_mul_f32 v252, v252, v251\n\t"
    "v_accvgpr_write_b32 a86, v252\n\t"
    "v_accvgpr_read_b32 v253, a87\n\t"
    "v_mul_f32 v253, v253, v251\n\t"
    "v_accvgpr_write_b32 a87, v253\n\t"
    "v_accvgpr_read_b32 v252, a88\n\t"
    "v_mul_f32 v252, v252, v251\n\t"
    "v_accvgpr_write_b32 a88, v252\n\t"
    "v_accvgpr_read_b32 v253, a89\n\t"
    "v_mul_f32 v253, v253, v251\n\t"
    "v_accvgpr_write_b32 a89, v253\n\t"
    "v_accvgpr_read_b32 v252, a90\n\t"
    "v_mul_f32 v252, v252, v251\n\t"
    "v_accvgpr_write_b32 a90, v252\n\t"
    "v_accvgpr_read_b32 v253, a91\n\t"
    "v_mul_f32 v253, v253, v251\n\t"
    "v_accvgpr_write_b32 a91, v253\n\t"
    "v_accvgpr_read_b32 v252, a92\n\t"
    "v_mul_f32 v252, v252, v251\n\t"
    "v_accvgpr_write_b32 a92, v252\n\t"
    "v_accvgpr_read_b32 v253, a93\n\t"
    "v_mul_f32 v253, v253, v251\n\t"
    "v_accvgpr_write_b32 a93, v253\n\t"
    "v_accvgpr_read_b32 v252, a94\n\t"
    "v_mul_f32 v252, v252, v251\n\t"
    "v_accvgpr_write_b32 a94, v252\n\t"
    "v_accvgpr_read_b32 v253, a95\n\t"
    "v_mul_f32 v253, v253, v251\n\t"
    "v_accvgpr_write_b32 a95, v253\n\t"
    "v_accvgpr_read_b32 v252, a96\n\t"
    "v_mul_f32 v252, v252, v250\n\t"
    "v_accvgpr_write_b32 a96, v252\n\t"
    "v_accvgpr_read_b32 v253, a97\n\t"
    "v_mul_f32 v253, v253, v250\n\t"
    "v_accvgpr_write_b32 a97, v253\n\t"
    "v_accvgpr_read_b32 v252, a98\n\t"
    "v_mul_f32 v252, v252, v250\n\t"
    "v_accvgpr_write_b32 a98, v252\n\t"
    "v_accvgpr_read_b32 v253, a99\n\t"
    "v_mul_f32 v253, v253, v250\n\t"
    "v_accvgpr_write_b32 a99, v253\n\t"
    "v_accvgpr_read_b32 v252, a100\n\t"
    "v_mul_f32 v252, v252, v250\n\t"
    "v_accvgpr_write_b32 a100, v252\n\t"
    "v_accvgpr_read_b32 v253, a101\n\t"
    "v_mul_f32 v253, v253, v250\n\t"
    "v_accvgpr_write_b32 a101, v253\n\t"
    "v_accvgpr_read_b32 v252, a102\n\t"
    "v_mul_f32 v252, v252, v250\n\t"
    "v_accvgpr_write_b32 a102, v252\n\t"
    "v_accvgpr_read_b32 v253, a103\n\t"
    "v_mul_f32 v253, v253, v250\n\t"
    "v_accvgpr_write_b32 a103, v253\n\t"
    "v_accvgpr_read_b32 v252, a104\n\t"
    "v_mul_f32 v252, v252, v250\n\t"
    "v_accvgpr_write_b32 a104, v252\n\t"
    "v_accvgpr_read_b32 v253, a105\n\t"
    "v_mul_f32 v253, v253, v250\n\t"
    "v_accvgpr_write_b32 a105, v253\n\t"
    "v_accvgpr_read_b32 v252, a106\n\t"
    "v_mul_f32 v252, v252, v250\n\t"
    "v_accvgpr_write_b32 a106, v252\n\t"
    "v_accvgpr_read_b32 v253, a107\n\t"
    "v_mul_f32 v253, v253, v250\n\t"
    "v_accvgpr_write_b32 a107, v253\n\t"
    "v_accvgpr_read_b32 v252, a108\n\t"
    "v_mul_f32 v252, v252, v250\n\t"
    "v_accvgpr_write_b32 a108, v252\n\t"
    "v_accvgpr_read_b32 v253, a109\n\t"
    "v_mul_f32 v253, v253, v250\n\t"
    "v_accvgpr_write_b32 a109, v253\n\t"
    "v_accvgpr_read_b32 v252, a110\n\t"
    "v_mul_f32 v252, v252, v250\n\t"
    "v_accvgpr_write_b32 a110, v252\n\t"
    "v_accvgpr_read_b32 v253, a111\n\t"
    "v_mul_f32 v253, v253, v250\n\t"
    "v_accvgpr_write_b32 a111, v253\n\t"
    "v_accvgpr_read_b32 v252, a112\n\t"
    "v_mul_f32 v252, v252, v251\n\t"
    "v_accvgpr_write_b32 a112, v252\n\t"
    "v_accvgpr_read_b32 v253, a113\n\t"
    "v_mul_f32 v253, v253, v251\n\t"
    "v_accvgpr_write_b32 a113, v253\n\t"
    "v_accvgpr_read_b32 v252, a114\n\t"
    "v_mul_f32 v252, v252, v251\n\t"
    "v_accvgpr_write_b32 a114, v252\n\t"
    "v_accvgpr_read_b32 v253, a115\n\t"
    "v_mul_f32 v253, v253, v251\n\t"
    "v_accvgpr_write_b32 a115, v253\n\t"
    "v_accvgpr_read_b32 v252, a116\n\t"
    "v_mul_f32 v252, v252, v251\n\t"
    "v_accvgpr_write_b32 a116, v252\n\t"
    "v_accvgpr_read_b32 v253, a117\n\t"
    "v_mul_f32 v253, v253, v251\n\t"
    "v_accvgpr_write_b32 a117, v253\n\t"
    "v_accvgpr_read_b32 v252, a118\n\t"
    "v_mul_f32 v252, v252, v251\n\t"
    "v_accvgpr_write_b32 a118, v252\n\t"
    "v_accvgpr_read_b32 v253, a119\n\t"
    "v_mul_f32 v253, v253, v251\n\t"
    "v_accvgpr_write_b32 a119, v253\n\t"
    "v_accvgpr_read_b32 v252, a120\n\t"
    "v_mul_f32 v252, v252, v251\n\t"
    "v_accvgpr_write_b32 a120, v252\n\t"
    "v_accvgpr_read_b32 v253, a121\n\t"
    "v_mul_f32 v253, v253, v251\n\t"
    "v_accvgpr_write_b32 a121, v253\n\t"
    "v_accvgpr_read_b32 v252, a122\n\t"
    "v_mul_f32 v252, v252, v251\n\t"
    "v_accvgpr_write_b32 a122, v252\n\t"
    "v_accvgpr_read_b32 v253, a123\n\t"
    "v_mul_f32 v253, v253, v251\n\t"
    "v_accvgpr_write_b32 a123, v253\n\t"
    "v_accvgpr_read_b32 v252, a124\n\t"
    "v_mul_f32 v252, v252, v251\n\t"
    "v_accvgpr_write_b32 a124, v252\n\t"
    "v_accvgpr_read_b32 v253, a125\n\t"
    "v_mul_f32 v253, v253, v251\n\t"
    "v_accvgpr_write_b32 a125, v253\n\t"
    "v_accvgpr_read_b32 v252, a126\n\t"
    "v_mul_f32 v252, v252, v251\n\t"
    "v_accvgpr_write_b32 a126, v252\n\t"
    "v_accvgpr_read_b32 v253, a127\n\t"
    "v_mul_f32 v253, v253, v251\n\t"
    "v_accvgpr_write_b32 a127, v253\n\t"
    "s_mov_b32 s46, 0\n\t"
    "s_nop 7\n\t"
    "s_branch RARE2R_3%=\n\t"
    "RARE1_4%=:\n\t"
    "s_nop 7\n\t"
    "s_nop 7\n\t"
    "v_mov_b32 v252, v248\n\t"
    "s_nop 1\n\t"
    "v_permlane32_swap_b32 v248, v252\n\t"
    "v_max_f32 v248, v248, v252\n\t"
    "v_max_f32 v253, 0, v248\n\t"
    "v_add_f32 v242, v242, v253\n\t"
    "v_sub_f32 v250, 0, v253\n\t"
    "v_exp_f32 v250, v250\n\t"
    "s_nop 0\n\t"
    "v_mul_f32 v244, v244, v250\n\t"
    "v_mul_f32 v245, v245, v250\n\t"
    "v_sub_f32 v0, v0, v253\n\t"
    "v_sub_f32 v1, v1, v253\n\t"
    "v_sub_f32 v2, v2, v253\n\t"
    "v_sub_f32 v3, v3, v253\n\t"
    "v_sub_f32 v4, v4, v253\n\t"
    "v_sub_f32 v5, v5, v253\n\t"
    "v_sub_f32 v6, v6, v253\n\t"
    "v_sub_f32 v7, v7, v253\n\t"
    "v_sub_f32 v8, v8, v253\n\t"
    "v_sub_f32 v9, v9, v253\n\t"
    "v_sub_f32 v10, v10, v253\n\t"
    "v_sub_f32 v11, v11, v253\n\t"
    "v_sub_f32 v12, v12, v253\n\t"
    "v_sub_f32 v13, v13, v253\n\t"
    "v_sub_f32 v14, v14, v253\n\t"
    "v_sub_f32 v15, v15, v253\n\t"
    "v_sub_f32 v32, v32, v253\n\t"
    "v_sub_f32 v33, v33, v253\n\t"
    "v_sub_f32 v34, v34, v253\n\t"
    "v_sub_f32 v35, v35, v253\n\t"
    "v_sub_f32 v36, v36, v253\n\t"
    "v_sub_f32 v37, v37, v253\n\t"
    "v_sub_f32 v38, v38, v253\n\t"
    "v_sub_f32 v39, v39, v253\n\t"
    "v_sub_f32 v40, v40, v253\n\t"
    "v_sub_f32 v41, v41, v253\n\t"
    "v_sub_f32 v42, v42, v253\n\t"
    "v_sub_f32 v43, v43, v253\n\t"
    "v_sub_f32 v44, v44, v253\n\t"
    "v_sub_f32 v45, v45, v253\n\t"
    "v_sub_f32 v46, v46, v253\n\t"
    "v_sub_f32 v47, v47, v253\n\t"
    "v_sub_f32 v252, 0, v242\n\t"
    "v_mov_b32 v192, v252\n\t"
    "v_mov_b32 v193, v252\n\t"
    "v_mov_b32 v194, v252\n\t"
    "v_mov_b32 v195, v252\n\t"
    "v_mov_b32 v196, v252\n\t"
    "v_mov_b32 v197, v252\n\t"
    "v_mov_b32 v198, v252\n\t"
    "v_mov_b32 v199, v252\n\t"
    "v_mov_b32 v200, v252\n\t"
    "v_mov_b32 v201, v252\n\t"
    "v_mov_b32 v202, v252\n\t"
    "v_mov_b32 v203, v252\n\t"
    "v_mov_b32 v204, v252\n\t"
    "v_mov_b32 v205, v252\n\t"
    "v_mov_b32 v206, v252\n\t"
    "v_mov_b32 v207, v252\n\t"
    "v_mov_b32 v252, v249\n\t"
    "s_nop 1\n\t"
    "v_permlane32_swap_b32 v249, v252\n\t"
    "v_max_f32 v249, v249, v252\n\t"
    "v_max_f32 v253, 0, v249\n\t"
    "v_add_f32 v243, v243, v253\n\t"
    "v_sub_f32 v251, 0, v253\n\t"
    "v_exp_f32 v251, v251\n\t"
    "s_nop 0\n\t"
    "v_mul_f32 v246, v246, v251\n\t"
    "v_mul_f32 v247, v247, v251\n\t"
    "v_sub_f32 v16, v16, v253\n\t"
    "v_sub_f32 v17, v17, v253\n\t"
    "v_sub_f32 v18, v18, v253\n\t"
    "v_sub_f32 v19, v19, v253\n\t"
    "v_sub_f32 v20, v20, v253\n\t"
    "v_sub_f32 v21, v21, v253\n\t"
    "v_sub_f32 v22, v22, v253\n\t"
    "v_sub_f32 v23, v23, v253\n\t"
    "v_sub_f32 v24, v24, v253\n\t"
    "v_sub_f32 v25, v25, v253\n\t"
    "v_sub_f32 v26, v26, v253\n\t"
    "v_sub_f32 v27, v27, v253\n\t"
    "v_sub_f32 v28, v28, v253\n\t"
    "v_sub_f32 v29, v29, v253\n\t"
    "v_sub_f32 v30, v30, v253\n\t"
    "v_sub_f32 v31, v31, v253\n\t"
    "v_sub_f32 v48, v48, v253\n\t"
    "v_sub_f32 v49, v49, v253\n\t"
    "v_sub_f32 v50, v50, v253\n\t"
    "v_sub_f32 v51, v51, v253\n\t"
    "v_sub_f32 v52, v52, v253\n\t"
    "v_sub_f32 v53, v53, v253\n\t"
    "v_sub_f32 v54, v54, v253\n\t"
    "v_sub_f32 v55, v55, v253\n\t"
    "v_sub_f32 v56, v56, v253\n\t"
    "v_sub_f32 v57, v57, v253\n\t"
    "v_sub_f32 v58, v58, v253\n\t"
    "v_sub_f32 v59, v59, v253\n\t"
    "v_sub_f32 v60, v60, v253\n\t"
    "v_sub_f32 v61, v61, v253\n\t"
    "v_sub_f32 v62, v62, v253\n\t"
    "v_sub_f32 v63, v63, v253\n\t"
    "v_sub_f32 v252, 0, v243\n\t"
    "v_mov_b32 v208, v252\n\t"
    "v_mov_b32 v209, v252\n\t"
    "v_mov_b32 v210, v252\n\t"
    "v_mov_b32 v211, v252\n\t"
    "v_mov_b32 v212, v252\n\t"
    "v_mov_b32 v213, v252\n\t"
    "v_mov_b32 v214, v252\n\t"
    "v_mov_b32 v215, v252\n\t"
    "v_mov_b32 v216, v252\n\t"
    "v_mov_b32 v217, v252\n\t"
    "v_mov_b32 v218, v252\n\t"
    "v_mov_b32 v219, v252\n\t"
    "v_mov_b32 v220, v252\n\t"
    "v_mov_b32 v221, v252\n\t"
    "v_mov_b32 v222, v252\n\t"
    "v_mov_b32 v223, v252\n\t"
    "s_mov_b32 s46, 1\n\t"
    "s_nop 7\n\t"
    "s_branch RARE1R_4%=\n\t"
    "RARE2_4%=:\n\t"
    "s_nop 7\n\t"
    "s_nop 7\n\t"
    "v_accvgpr_read_b32 v252, a0\n\t"
    "v_mul_f32 v252, v252, v250\n\t"
    "v_accvgpr_write_b32 a0, v252\n\t"
    "v_accvgpr_read_b32 v253, a1\n\t"
    "v_mul_f32 v253, v253, v250\n\t"
    "v_accvgpr_write_b32 a1, v253\n\t"
    "v_accvgpr_read_b32 v252, a2\n\t"
    "v_mul_f32 v252, v252, v250\n\t"
    "v_accvgpr_write_b32 a2, v252\n\t"
    "v_accvgpr_read_b32 v253, a3\n\t"
    "v_mul_f32 v253, v253, v250\n\t"
    "v_accvgpr_write_b32 a3, v253\n\t"
    "v_accvgpr_read_b32 v252, a4\n\t"
    "v_mul_f32 v252, v252, v250\n\t"
    "v_accvgpr_write_b32 a4, v252\n\t"
    "v_accvgpr_read_b32 v253, a5\n\t"
    "v_mul_f32 v253, v253, v250\n\t"
    "v_accvgpr_write_b32 a5, v253\n\t"
    "v_accvgpr_read_b32 v252, a6\n\t"
    "v_mul_f32 v252, v252, v250\n\t"
    "v_accvgpr_write_b32 a6, v252\n\t"
    "v_accvgpr_read_b32 v253, a7\n\t"
    "v_mul_f32 v253, v253, v250\n\t"
    "v_accvgpr_write_b32 a7, v253\n\t"
    "v_accvgpr_read_b32 v252, a8\n\t"
    "v_mul_f32 v252, v252, v250\n\t"
    "v_accvgpr_write_b32 a8, v252\n\t"
    "v_accvgpr_read_b32 v253, a9\n\t"
    "v_mul_f32 v253, v253, v250\n\t"
    "v_accvgpr_write_b32 a9, v253\n\t"
    "v_accvgpr_read_b32 v252, a10\n\t"
    "v_mul_f32 v252, v252, v250\n\t"
    "v_accvgpr_write_b32 a10, v252\n\t"
    "v_accvgpr_read_b32 v253, a11\n\t"
    "v_mul_f32 v253, v253, v250\n\t"
    "v_accvgpr_write_b32 a11, v253\n\t"
    "v_accvgpr_read_b32 v252, a12\n\t"
    "v_mul_f32 v252, v252, v250\n\t"
    "v_accvgpr_write_b32 a12, v252\n\t"
    "v_accvgpr_read_b32 v253, a13\n\t"
    "v_mul_f32 v253, v253, v250\n\t"
    "v_accvgpr_write_b32 a13, v253\n\t"
    "v_accvgpr_read_b32 v252, a14\n\t"
    "v_mul_f32 v252, v252, v250\n\t"
    "v_accvgpr_write_b32 a14, v252\n\t"
    "v_accvgpr_read_b32 v253, a15\n\t"
    "v_mul_f32 v253, v253, v250\n\t"
    "v_accvgpr_write_b32 a15, v253\n\t"
    "v_accvgpr_read_b32 v252, a16\n\t"
    "v_mul_f32 v252, v252, v251\n\t"
    "v_accvgpr_write_b32 a16, v252\n\t"
    "v_accvgpr_read_b32 v253, a17\n\t"
    "v_mul_f32 v253, v253, v251\n\t"
    "v_accvgpr_write_b32 a17, v253\n\t"
    "v_accvgpr_read_b32 v252, a18\n\t"
    "v_mul_f32 v252, v252, v251\n\t"
    "v_accvgpr_write_b32 a18, v252\n\t"
    "v_accvgpr_read_b32 v253, a19\n\t"
    "v_mul_f32 v253, v253, v251\n\t"
    "v_accvgpr_write_b32 a19, v253\n\t"
    "v_accvgpr_read_b32 v252, a20\n\t"
    "v_mul_f32 v252, v252, v251\n\t"
    "v_accvgpr_write_b32 a20, v252\n\t"
    "v_accvgpr_read_b32 v253, a21\n\t"
    "v_mul_f32 v253, v253, v251\n\t"
    "v_accvgpr_write_b32 a21, v253\n\t"
    "v_accvgpr_read_b32 v252, a22\n\t"
    "v_mul_f32 v252, v252, v251\n\t"
    "v_accvgpr_write_b32 a22, v252\n\t"
    "v_accvgpr_read_b32 v253, a23\n\t"
    "v_mul_f32 v253, v253, v251\n\t"
    "v_accvgpr_write_b32 a23, v253\n\t"
    "v_accvgpr_read_b32 v252, a24\n\t"
    "v_mul_f32 v252, v252, v251\n\t"
    "v_accvgpr_write_b32 a24, v252\n\t"
    "v_accvgpr_read_b32 v253, a25\n\t"
    "v_mul_f32 v253, v253, v251\n\t"
    "v_accvgpr_write_b32 a25, v253\n\t"
    "v_accvgpr_read_b32 v252, a26\n\t"
    "v_mul_f32 v252, v252, v251\n\t"
    "v_accvgpr_write_b32 a26, v252\n\t"
    "v_accvgpr_read_b32 v253, a27\n\t"
    "v_mul_f32 v253, v253, v251\n\t"
    "v_accvgpr_write_b32 a27, v253\n\t"
    "v_accvgpr_read_b32 v252, a28\n\t"
    "v_mul_f32 v252, v252, v251\n\t"
    "v_accvgpr_write_b32 a28, v252\n\t"
    "v_accvgpr_read_b32 v253, a29\n\t"
    "v_mul_f32 v253, v253, v251\n\t"
    "v_accvgpr_write_b32 a29, v253\n\t"
    "v_accvgpr_read_b32 v252, a30\n\t"
    "v_mul_f32 v252, v252, v251\n\t"
    "v_accvgpr_write_b32 a30, v252\n\t"
    "v_accvgpr_read_b32 v253, a31\n\t"
    "v_mul_f32 v253, v253, v251\n\t"
    "v_accvgpr_write_b32 a31, v253\n\t"
    "v_accvgpr_read_b32 v252, a32\n\t"
    "v_mul_f32 v252, v252, v250\n\t"
    "v_accvgpr_write_b32 a32, v252\n\t"
    "v_accvgpr_read_b32 v253, a33\n\t"
    "v_mul_f32 v253, v253, v250\n\t"
    "v_accvgpr_write_b32 a33, v253\n\t"
    "v_accvgpr_read_b32 v252, a34\n\t"
    "v_mul_f32 v252, v252, v250\n\t"
    "v_accvgpr_write_b32 a34, v252\n\t"
    "v_accvgpr_read_b32 v253, a35\n\t"
    "v_mul_f32 v253, v253, v250\n\t"
    "v_accvgpr_write_b32 a35, v253\n\t"
    "v_accvgpr_read_b32 v252, a36\n\t"
    "v_mul_f32 v252, v252, v250\n\t"
    "v_accvgpr_write_b32 a36, v252\n\t"
    "v_accvgpr_read_b32 v253, a37\n\t"
    "v_mul_f32 v253, v253, v250\n\t"
    "v_accvgpr_write_b32 a37, v253\n\t"
    "v_accvgpr_read_b32 v252, a38\n\t"
    "v_mul_f32 v252, v252, v250\n\t"
    "v_accvgpr_write_b32 a38, v252\n\t"
    "v_accvgpr_read_b32 v253, a39\n\t"
    "v_mul_f32 v253, v253, v250\n\t"
    "v_accvgpr_write_b32 a39, v253\n\t"
    "v_accvgpr_read_b32 v252, a40\n\t"
    "v_mul_f32 v252, v252, v250\n\t"
    "v_accvgpr_write_b32 a40, v252\n\t"
    "v_accvgpr_read_b32 v253, a41\n\t"
    "v_mul_f32 v253, v253, v250\n\t"
    "v_accvgpr_write_b32 a41, v253\n\t"
    "v_accvgpr_read_b32 v252, a42\n\t"
    "v_mul_f32 v252, v252, v250\n\t"
    "v_accvgpr_write_b32 a42, v252\n\t"
    "v_accvgpr_read_b32 v253, a43\n\t"
    "v_mul_f32 v253, v253, v250\n\t"
    "v_accvgpr_write_b32 a43, v253\n\t"
    "v_accvgpr_read_b32 v252, a44\n\t"
    "v_mul_f32 v252, v252, v250\n\t"
    "v_accvgpr_write_b32 a44, v252\n\t"
    "v_accvgpr_read_b32 v253, a45\n\t"
    "v_mul_f32 v253, v253, v250\n\t"
    "v_accvgpr_write_b32 a45, v253\n\t"
    "v_accvgpr_read_b32 v252, a46\n\t"
    "v_mul_f32 v252, v252, v250\n\t"
    "v_accvgpr_write_b32 a46, v252\n\t"
    "v_accvgpr_read_b32 v253, a47\n\t"
    "v_mul_f32 v253, v253, v250\n\t"
    "v_accvgpr_write_b32 a47, v253\n\t"
    "v_accvgpr_read_b32 v252, a48\n\t"
    "v_mul_f32 v252, v252, v251\n\t"
    "v_accvgpr_write_b32 a48, v252\n\t"
    "v_accvgpr_read_b32 v253, a49\n\t"
    "v_mul_f32 v253, v253, v251\n\t"
    "v_accvgpr_write_b32 a49, v253\n\t"
    "v_accvgpr_read_b32 v252, a50\n\t"
    "v_mul_f32 v252, v252, v251\n\t"
    "v_accvgpr_write_b32 a50, v252\n\t"
    "v_accvgpr_read_b32 v253, a51\n\t"
    "v_mul_f32 v253, v253, v251\n\t"
    "v_accvgpr_write_b32 a51, v253\n\t"
    "v_accvgpr_read_b32 v252, a52\n\t"
    "v_mul_f32 v252, v252, v251\n\t"
    "v_accvgpr_write_b32 a52, v252\n\t"
    "v_accvgpr_read_b32 v253, a53\n\t"
    "v_mul_f32 v253, v253, v251\n\t"
    "v_accvgpr_write_b32 a53, v253\n\t"
    "v_accvgpr_read_b32 v252, a54\n\t"
    "v_mul_f32 v252, v252, v251\n\t"
    "v_accvgpr_write_b32 a54, v252\n\t"
    "v_accvgpr_read_b32 v253, a55\n\t"
    "v_mul_f32 v253, v253, v251\n\t"
    "v_accvgpr_write_b32 a55, v253\n\t"
    "v_accvgpr_read_b32 v252, a56\n\t"
    "v_mul_f32 v252, v252, v251\n\t"
    "v_accvgpr_write_b32 a56, v252\n\t"
    "v_accvgpr_read_b32 v253, a57\n\t"
    "v_mul_f32 v253, v253, v251\n\t"
    "v_accvgpr_write_b32 a57, v253\n\t"
    "v_accvgpr_read_b32 v252, a58\n\t"
    "v_mul_f32 v252, v252, v251\n\t"
    "v_accvgpr_write_b32 a58, v252\n\t"
    "v_accvgpr_read_b32 v253, a59\n\t"
    "v_mul_f32 v253, v253, v251\n\t"
    "v_accvgpr_write_b32 a59, v253\n\t"
    "v_accvgpr_read_b32 v252, a60\n\t"
    "v_mul_f32 v252, v252, v251\n\t"
    "v_accvgpr_write_b32 a60, v252\n\t"
    "v_accvgpr_read_b32 v253, a61\n\t"
    "v_mul_f32 v253, v253, v251\n\t"
    "v_accvgpr_write_b32 a61, v253\n\t"
    "v_accvgpr_read_b32 v252, a62\n\t"
    "v_mul_f32 v252, v252, v251\n\t"
    "v_accvgpr_write_b32 a62, v252\n\t"
    "v_accvgpr_read_b32 v253, a63\n\t"
    "v_mul_f32 v253, v253, v251\n\t"
    "v_accvgpr_write_b32 a63, v253\n\t"
    "v_accvgpr_read_b32 v252, a64\n\t"
    "v_mul_f32 v252, v252, v250\n\t"
    "v_accvgpr_write_b32 a64, v252\n\t"
    "v_accvgpr_read_b32 v253, a65\n\t"
    "v_mul_f32 v253, v253, v250\n\t"
    "v_accvgpr_write_b32 a65, v253\n\t"
    "v_accvgpr_read_b32 v252, a66\n\t"
    "v_mul_f32 v252, v252, v250\n\t"
    "v_accvgpr_write_b32 a66, v252\n\t"
    "v_accvgpr_read_b32 v253, a67\n\t"
    "v_mul_f32 v253, v253, v250\n\t"
    "v_accvgpr_write_b32 a67, v253\n\t"
    "v_accvgpr_read_b32 v252, a68\n\t"
    "v_mul_f32 v252, v252, v250\n\t"
    "v_accvgpr_write_b32 a68, v252\n\t"
    "v_accvgpr_read_b32 v253, a69\n\t"
    "v_mul_f32 v253, v253, v250\n\t"
    "v_accvgpr_write_b32 a69, v253\n\t"
    "v_accvgpr_read_b32 v252, a70\n\t"
    "v_mul_f32 v252, v252, v250\n\t"
    "v_accvgpr_write_b32 a70, v252\n\t"
    "v_accvgpr_read_b32 v253, a71\n\t"
    "v_mul_f32 v253, v253, v250\n\t"
    "v_accvgpr_write_b32 a71, v253\n\t"
    "v_accvgpr_read_b32 v252, a72\n\t"
    "v_mul_f32 v252, v252, v250\n\t"
    "v_accvgpr_write_b32 a72, v252\n\t"
    "v_accvgpr_read_b32 v253, a73\n\t"
    "v_mul_f32 v253, v253, v250\n\t"
    "v_accvgpr_write_b32 a73, v253\n\t"
    "v_accvgpr_read_b32 v252, a74\n\t"
    "v_mul_f32 v252, v252, v250\n\t"
    "v_accvgpr_write_b32 a74, v252\n\t"
    "v_accvgpr_read_b32 v253, a75\n\t"
    "v_mul_f32 v253, v253, v250\n\t"
    "v_accvgpr_write_b32 a75, v253\n\t"
    "v_accvgpr_read_b32 v252, a76\n\t"
    "v_mul_f32 v252, v252, v250\n\t"
    "v_accvgpr_write_b32 a76, v252\n\t"
    "v_accvgpr_read_b32 v253, a77\n\t"
    "v_mul_f32 v253, v253, v250\n\t"
    "v_accvgpr_write_b32 a77, v253\n\t"
    "v_accvgpr_read_b32 v252, a78\n\t"
    "v_mul_f32 v252, v252, v250\n\t"
    "v_accvgpr_write_b32 a78, v252\n\t"
    "v_accvgpr_read_b32 v253, a79\n\t"
    "v_mul_f32 v253, v253, v250\n\t"
    "v_accvgpr_write_b32 a79, v253\n\t"
    "v_accvgpr_read_b32 v252, a80\n\t"
    "v_mul_f32 v252, v252, v251\n\t"
    "v_accvgpr_write_b32 a80, v252\n\t"
    "v_accvgpr_read_b32 v253, a81\n\t"
    "v_mul_f32 v253, v253, v251\n\t"
    "v_accvgpr_write_b32 a81, v253\n\t"
    "v_accvgpr_read_b32 v252, a82\n\t"
    "v_mul_f32 v252, v252, v251\n\t"
    "v_accvgpr_write_b32 a82, v252\n\t"
    "v_accvgpr_read_b32 v253, a83\n\t"
    "v_mul_f32 v253, v253, v251\n\t"
    "v_accvgpr_write_b32 a83, v253\n\t"
    "v_accvgpr_read_b32 v252, a84\n\t"
    "v_mul_f32 v252, v252, v251\n\t"
    "v_accvgpr_write_b32 a84, v252\n\t"
    "v_accvgpr_read_b32 v253, a85\n\t"
    "v_mul_f32 v253, v253, v251\n\t"
    "v_accvgpr_write_b32 a85, v253\n\t"
    "v_accvgpr_read_b32 v252, a86\n\t"
    "v_mul_f32 v252, v252, v251\n\t"
    "v_accvgpr_write_b32 a86, v252\n\t"
    "v_accvgpr_read_b32 v253, a87\n\t"
    "v_mul_f32 v253, v253, v251\n\t"
    "v_accvgpr_write_b32 a87, v253\n\t"
    "v_accvgpr_read_b32 v252, a88\n\t"
    "v_mul_f32 v252, v252, v251\n\t"
    "v_accvgpr_write_b32 a88, v252\n\t"
    "v_accvgpr_read_b32 v253, a89\n\t"
    "v_mul_f32 v253, v253, v251\n\t"
    "v_accvgpr_write_b32 a89, v253\n\t"
    "v_accvgpr_read_b32 v252, a90\n\t"
    "v_mul_f32 v252, v252, v251\n\t"
    "v_accvgpr_write_b32 a90, v252\n\t"
    "v_accvgpr_read_b32 v253, a91\n\t"
    "v_mul_f32 v253, v253, v251\n\t"
    "v_accvgpr_write_b32 a91, v253\n\t"
    "v_accvgpr_read_b32 v252, a92\n\t"
    "v_mul_f32 v252, v252, v251\n\t"
    "v_accvgpr_write_b32 a92, v252\n\t"
    "v_accvgpr_read_b32 v253, a93\n\t"
    "v_mul_f32 v253, v253, v251\n\t"
    "v_accvgpr_write_b32 a93, v253\n\t"
    "v_accvgpr_read_b32 v252, a94\n\t"
    "v_mul_f32 v252, v252, v251\n\t"
    "v_accvgpr_write_b32 a94, v252\n\t"
    "v_accvgpr_read_b32 v253, a95\n\t"
    "v_mul_f32 v253, v253, v251\n\t"
    "v_accvgpr_write_b32 a95, v253\n\t"
    "v_accvgpr_read_b32 v252, a96\n\t"
    "v_mul_f32 v252, v252, v250\n\t"
    "v_accvgpr_write_b32 a96, v252\n\t"
    "v_accvgpr_read_b32 v253, a97\n\t"
    "v_mul_f32 v253, v253, v250\n\t"
    "v_accvgpr_write_b32 a97, v253\n\t"
    "v_accvgpr_read_b32 v252, a98\n\t"
    "v_mul_f32 v252, v252, v250\n\t"
    "v_accvgpr_write_b32 a98, v252\n\t"
    "v_accvgpr_read_b32 v253, a99\n\t"
    "v_mul_f32 v253, v253, v250\n\t"
    "v_accvgpr_write_b32 a99, v253\n\t"
    "v_accvgpr_read_b32 v252, a100\n\t"
    "v_mul_f32 v252, v252, v250\n\t"
    "v_accvgpr_write_b32 a100, v252\n\t"
    "v_accvgpr_read_b32 v253, a101\n\t"
    "v_mul_f32 v253, v253, v250\n\t"
    "v_accvgpr_write_b32 a101, v253\n\t"
    "v_accvgpr_read_b32 v252, a102\n\t"
    "v_mul_f32 v252, v252, v250\n\t"
    "v_accvgpr_write_b32 a102, v252\n\t"
    "v_accvgpr_read_b32 v253, a103\n\t"
    "v_mul_f32 v253, v253, v250\n\t"
    "v_accvgpr_write_b32 a103, v253\n\t"
    "v_accvgpr_read_b32 v252, a104\n\t"
    "v_mul_f32 v252, v252, v250\n\t"
    "v_accvgpr_write_b32 a104, v252\n\t"
    "v_accvgpr_read_b32 v253, a105\n\t"
    "v_mul_f32 v253, v253, v250\n\t"
    "v_accvgpr_write_b32 a105, v253\n\t"
    "v_accvgpr_read_b32 v252, a106\n\t"
    "v_mul_f32 v252, v252, v250\n\t"
    "v_accvgpr_write_b32 a106, v252\n\t"
    "v_accvgpr_read_b32 v253, a107\n\t"
    "v_mul_f32 v253, v253, v250\n\t"
    "v_accvgpr_write_b32 a107, v253\n\t"
    "v_accvgpr_read_b32 v252, a108\n\t"
    "v_mul_f32 v252, v252, v250\n\t"
    "v_accvgpr_write_b32 a108, v252\n\t"
    "v_accvgpr_read_b32 v253, a109\n\t"
    "v_mul_f32 v253, v253, v250\n\t"
    "v_accvgpr_write_b32 a109, v253\n\t"
    "v_accvgpr_read_b32 v252, a110\n\t"
    "v_mul_f32 v252, v252, v250\n\t"
    "v_accvgpr_write_b32 a110, v252\n\t"
    "v_accvgpr_read_b32 v253, a111\n\t"
    "v_mul_f32 v253, v253, v250\n\t"
    "v_accvgpr_write_b32 a111, v253\n\t"
    "v_accvgpr_read_b32 v252, a112\n\t"
    "v_mul_f32 v252, v252, v251\n\t"
    "v_accvgpr_write_b32 a112, v252\n\t"
    "v_accvgpr_read_b32 v253, a113\n\t"
    "v_mul_f32 v253, v253, v251\n\t"
    "v_accvgpr_write_b32 a113, v253\n\t"
    "v_accvgpr_read_b32 v252, a114\n\t"
    "v_mul_f32 v252, v252, v251\n\t"
    "v_accvgpr_write_b32 a114, v252\n\t"
    "v_accvgpr_read_b32 v253, a115\n\t"
    "v_mul_f32 v253, v253, v251\n\t"
    "v_accvgpr_write_b32 a115, v253\n\t"
    "v_accvgpr_read_b32 v252, a116\n\t"
    "v_mul_f32 v252, v252, v251\n\t"
    "v_accvgpr_write_b32 a116, v252\n\t"
    "v_accvgpr_read_b32 v253, a117\n\t"
    "v_mul_f32 v253, v253, v251\n\t"
    "v_accvgpr_write_b32 a117, v253\n\t"
    "v_accvgpr_read_b32 v252, a118\n\t"
    "v_mul_f32 v252, v252, v251\n\t"
    "v_accvgpr_write_b32 a118, v252\n\t"
    "v_accvgpr_read_b32 v253, a119\n\t"
    "v_mul_f32 v253, v253, v251\n\t"
    "v_accvgpr_write_b32 a119, v253\n\t"
    "v_accvgpr_read_b32 v252, a120\n\t"
    "v_mul_f32 v252, v252, v251\n\t"
    "v_accvgpr_write_b32 a120, v252\n\t"
    "v_accvgpr_read_b32 v253, a121\n\t"
    "v_mul_f32 v253, v253, v251\n\t"
    "v_accvgpr_write_b32 a121, v253\n\t"
    "v_accvgpr_read_b32 v252, a122\n\t"
    "v_mul_f32 v252, v252, v251\n\t"
    "v_accvgpr_write_b32 a122, v252\n\t"
    "v_accvgpr_read_b32 v253, a123\n\t"
    "v_mul_f32 v253, v253, v251\n\t"
    "v_accvgpr_write_b32 a123, v253\n\t"
    "v_accvgpr_read_b32 v252, a124\n\t"
    "v_mul_f32 v252, v252, v251\n\t"
    "v_accvgpr_write_b32 a124, v252\n\t"
    "v_accvgpr_read_b32 v253, a125\n\t"
    "v_mul_f32 v253, v253, v251\n\t"
    "v_accvgpr_write_b32 a125, v253\n\t"
    "v_accvgpr_read_b32 v252, a126\n\t"
    "v_mul_f32 v252, v252, v251\n\t"
    "v_accvgpr_write_b32 a126, v252\n\t"
    "v_accvgpr_read_b32 v253, a127\n\t"
    "v_mul_f32 v253, v253, v251\n\t"
    "v_accvgpr_write_b32 a127, v253\n\t"
    "s_mov_b32 s46, 0\n\t"
    "s_nop 7\n\t"
    "s_branch RARE2R_4%=\n\t"
    "END%=:\n\t"
    :: "s"(srdQ), "s"(srdK), "s"(srdV), "s"(srdO), "v"(tid)
    : "memory", "vcc", "m0", "scc", "v0", "v1", "v2", "v3", "v4", "v5", "v6", "v7", "v8", "v9", "v10", "v11", "v12", "v13", "v14", "v15", "v16", "v17", "v18", "v19", "v20", "v21", "v22", "v23", "v24", "v25", "v26", "v27", "v28", "v29", "v30", "v31", "v32", "v33", "v34", "v35", "v36", "v37", "v38", "v39", "v40", "v41", "v42", "v43", "v44", "v45", "v46", "v47", "v48", "v49", "v50", "v51", "v52", "v53", "v54", "v55", "v56", "v57", "v58", "v59", "v60", "v61", "v62", "v63", "v64", "v65", "v66", "v67", "v68", "v69", "v70", "v71", "v72", "v73", "v74", "v75", "v76", "v77", "v78", "v79", "v80", "v81", "v82", "v83", "v84", "v85", "v86", "v87", "v88", "v89", "v90", "v91", "v92", "v93", "v94", "v95", "v96", "v97", "v98", "v99", "v100", "v101", "v102", "v103", "v104", "v105", "v106", "v107", "v108", "v109", "v110", "v111", "v112", "v113", "v114", "v115", "v116", "v117", "v118", "v119", "v120", "v121", "v122", "v123", "v124", "v125", "v126", "v127", "v128", "v129", "v130", "v131", "v132", "v133", "v134", "v135", "v136", "v137", "v138", "v139", "v140", "v141", "v142", "v143", "v144", "v145", "v146", "v147", "v148", "v149", "v150", "v151", "v152", "v153", "v154", "v155", "v156", "v157", "v158", "v159", "v160", "v161", "v162", "v163", "v164", "v165", "v166", "v167", "v168", "v169", "v170", "v171", "v172", "v173", "v174", "v175", "v176", "v177", "v178", "v179", "v180", "v181", "v182", "v183", "v184", "v185", "v186", "v187", "v188", "v189", "v190", "v191", "v192", "v193", "v194", "v195", "v196", "v197", "v198", "v199", "v200", "v201", "v202", "v203", "v204", "v205", "v206", "v207", "v208", "v209", "v210", "v211", "v212", "v213", "v214", "v215", "v216", "v217", "v218", "v219", "v220", "v221", "v222", "v223", "v224", "v225", "v226", "v227", "v228", "v229", "v230", "v231", "v232", "v233", "v234", "v235", "v236", "v237", "v238", "v239", "v240", "v241", "v242", "v243", "v244", "v245", "v246", "v247", "v248", "v249", "v250", "v251", "v252", "v253", "v254", "a0", "a1", "a2", "a3", "a4", "a5", "a6", "a7", "a8", "a9", "a10", "a11", "a12", "a13", "a14", "a15", "a16", "a17", "a18", "a19", "a20", "a21", "a22", "a23", "a24", "a25", "a26", "a27", "a28", "a29", "a30", "a31", "a32", "a33", "a34", "a35", "a36", "a37", "a38", "a39", "a40", "a41", "a42", "a43", "a44", "a45", "a46", "a47", "a48", "a49", "a50", "a51", "a52", "a53", "a54", "a55", "a56", "a57", "a58", "a59", "a60", "a61", "a62", "a63", "a64", "a65", "a66", "a67", "a68", "a69", "a70", "a71", "a72", "a73", "a74", "a75", "a76", "a77", "a78", "a79", "a80", "a81", "a82", "a83", "a84", "a85", "a86", "a87", "a88", "a89", "a90", "a91", "a92", "a93", "a94", "a95", "a96", "a97", "a98", "a99", "a100", "a101", "a102", "a103", "a104", "a105", "a106", "a107", "a108", "a109", "a110", "a111", "a112", "a113", "a114", "a115", "a116", "a117", "a118", "a119", "a120", "a121", "a122", "a123", "a124", "a125", "a126", "a127", "a128", "a129", "a130", "a131", "a132", "a133", "a134", "a135", "a136", "a137", "a138", "a139", "a140", "a141", "a142", "a143", "a144", "a145", "a146", "a147", "a148", "a149", "a150", "a151", "a152", "a153", "a154", "a155", "a156", "a157", "a158", "a159", "a160", "a161", "a162", "a163", "a164", "a165", "a166", "a167", "a168", "a169", "a170", "a171", "a172", "a173", "a174", "a175", "a176", "a177", "a178", "a179", "a180", "a181", "a182", "a183", "a184", "a185", "a186", "a187", "a188", "a189", "a190", "a191", "a192", "a193", "a194", "a195", "a196", "a197", "a198", "a199", "a200", "a201", "a202", "a203", "a204", "a205", "a206", "a207", "a208", "a209", "a210", "a211", "a212", "a213", "a214", "a215", "a216", "a217", "a218", "a219", "a220", "a221", "a222", "a223", "a224", "a225", "a226", "a227", "a228", "a229", "a230", "a231", "a232", "a233", "a234", "a235", "a236", "a237", "a238", "a239", "a240", "a241", "a242", "a243", "a244", "a245", "a246", "a247", "a248", "a249", "a250", "a251", "a252", "a253", "a254", "a255", "s40", "s41", "s42", "s43", "s44", "s45", "s46", "s47", "s48", "s49", "s50", "s51", "s52", "s53", "s54", "s55", "s56", "s57", "s58", "s59", "s60", "s61", "s62", "s63", "s64", "s65", "s66", "s67", "s68", "s69", "s70", "s71", "s72", "s73", "s74", "s75", "s76", "s77", "s78", "s79", "s80", "s81", "s82", "s83", "s84", "s85", "s86", "s87", "s88", "s89", "s90", "s91", "s92", "s93", "s94", "s95", "s96", "s97", "s98", "s99");
}

extern "C" void kernel_launch(void* const* d_in, const int* in_sizes, int n_in, void* d_out, int out_size, void* d_ws, size_t ws_size,
                              hipStream_t stream) {
  hipLaunchKernelGGL(gqa_attn_kernel, dim3(256), dim3(256), 0, stream, (const unsigned short*)d_in[0], (const unsigned short*)d_in[1],
                     (const unsigned short*)d_in[2], (unsigned short*)d_out);
}
```
